```python
import torch

def _softmax(x, axis=-1):
    return torch.softmax(x, dim=axis)
def _gelu(x):
    return torch.nn.functional.gelu(x, approximate="tanh")
def _silu(x):
    return torch.nn.functional.silu(x)
def _sigmoid(x):
    return torch.sigmoid(x)
def _layernorm(x, axis=-1, eps=1e-5):
    m = x.mean(dim=axis, keepdim=True)
    v = x.var(dim=axis, keepdim=True, unbiased=False)
    return (x - m) / torch.sqrt(v + eps)
def _scatter_add(tgt, idx, src, axis=0):
    return tgt.clone().index_add_(axis, idx, src)
def _segment_sum(src, seg, n):
    out = torch.zeros((n,) + src.shape[1:], dtype=src.dtype)
    return out.index_add_(0, seg, src)
def _pad(x, pw):
    flat = tuple(v for pair in reversed(pw) for v in pair)
    return torch.nn.functional.pad(x, flat)

kernel_name = 'standalone__weight_only_int_gptq__prompt'

def setup_inputs():

    _w = (lambda a: (lambda t: t.to(torch.bfloat16) if t.is_floating_point() else t)(torch.as_tensor(a)))
    B, S, D, F = 1, 2048, 576, 4608
    bits = max(2, min(4, 4))
    pf = 32 // bits
    G = -1 if -1 > 0 else D
    G = max(pf, (G // pf) * pf)
    D = max(G, (D // G) * G)
    ng = D // G
    qmax = 1 << bits
    qw = torch.randint(0, qmax, (D, F)).to(torch.int64)
    packed = torch.zeros((D // pf, F), dtype=torch.int64)
    for i in range(pf):
        packed |= (qw[i::pf] << (i * bits))
    packed = packed.to(torch.int32)
    return {
        "x": _w((lambda *s: torch.randn(*s, dtype=torch.bfloat16) * 0.05)(B, S, D)),
        "qw_packed": _w(packed),
        "scales": _w(torch.abs((lambda *s: torch.randn(*s, dtype=torch.bfloat16) * 0.05)(ng, F)).to(float) * 0.1 + 0.01),
        "zeros": _w(torch.randint(0, qmax, (ng, F)).to(float)),
        "bits": _w((lambda _x: torch.as_tensor(_x, dtype=torch.int32))(bits)),
    }

def reference(x, qw_packed, scales, zeros, bits):
    b = int(bits)
    pf = 32 // b
    mask = (1 << b) - 1
    P, F = qw_packed.shape
    D = P * pf
    ng = scales.shape[0]
    G = D // ng
    shifts = (lambda x, dt: x.to(dt))(torch.arange(pf) * b, qw_packed.dtype)
    qw = ((qw_packed[:, None, :] >> shifts[None, :, None]) & mask).reshape(D, F)
    s = (lambda x, n, axis: torch.repeat_interleave(x, n, dim=axis))(scales, G, axis=0)
    z = (lambda x, n, axis: torch.repeat_interleave(x, n, dim=axis))(zeros, G, axis=0)
    w = ((lambda x, dt: x.to(dt))(qw, x.dtype) - z) * s
    return torch.einsum("bsd,df->bsf", x, w)
```

```cpp
#include <hip/hip_runtime.h>
#include <stdint.h>

typedef __bf16 bf16x8 __attribute__((ext_vector_type(8)));
typedef __bf16 bf16x2 __attribute__((ext_vector_type(2)));
typedef _Float16 f16x2 __attribute__((ext_vector_type(2)));
typedef float f32x16 __attribute__((ext_vector_type(16)));
typedef float f32x2 __attribute__((ext_vector_type(2)));
typedef unsigned u32x4 __attribute__((ext_vector_type(4)));
#define DI __device__ __forceinline__

constexpr int MM = 2048, KK = 576, NN = 4608;
constexpr int XROW = KK * 2;
constexpr int WROW = NN * 4;
constexpr int OROW = NN * 2;

#define STORE_AUX 17

template <int N> DI void wait_vm() { asm volatile("s_waitcnt vmcnt(%0)" ::"i"(N) : "memory"); }
DI void bar() { asm volatile("s_barrier" ::: "memory"); }

DI unsigned pack_bf16(float a, float b) {
  f32x2 v = {a, b};
  bf16x2 r = __builtin_convertvector(v, bf16x2);
  return __builtin_bit_cast(unsigned, r);
}

DI u32x4 dequant(unsigned p, float s24, float s20, float nzs) {
  unsigned p8 = p >> 8;
  f16x2 m0 = __builtin_bit_cast(f16x2, p & 0x000F000Fu);
  f16x2 m1 = __builtin_bit_cast(f16x2, p & 0x00F000F0u);
  f16x2 m2 = __builtin_bit_cast(f16x2, p8 & 0x000F000Fu);
  f16x2 m3 = __builtin_bit_cast(f16x2, p8 & 0x00F000F0u);
  u32x4 r;
  r[0] = pack_bf16(__builtin_fmaf((float)m0[0], s24, nzs), __builtin_fmaf((float)m1[0], s20, nzs));
  r[1] = pack_bf16(__builtin_fmaf((float)m2[0], s24, nzs), __builtin_fmaf((float)m3[0], s20, nzs));
  r[2] = pack_bf16(__builtin_fmaf((float)m0[1], s24, nzs), __builtin_fmaf((float)m1[1], s20, nzs));
  r[3] = pack_bf16(__builtin_fmaf((float)m2[1], s24, nzs), __builtin_fmaf((float)m3[1], s20, nzs));
  return r;
}

DI f32x16 mfma(u32x4 a, u32x4 b, f32x16 c) {
  return __builtin_amdgcn_mfma_f32_32x32x16_bf16(__builtin_bit_cast(bf16x8, a), __builtin_bit_cast(bf16x8, b), c, 0, 0, 0);
}

DI void epilogue(const f32x16& acc, __amdgpu_buffer_rsrc_t srdO, unsigned voff, unsigned soff) {
  u32x4 a, b;
#pragma unroll
  for (int j = 0; j < 4; ++j) {
    unsigned lo = pack_bf16(acc[2 * j], acc[2 * j + 1]);
    unsigned hi = pack_bf16(acc[8 + 2 * j], acc[8 + 2 * j + 1]);
    auto r = __builtin_amdgcn_permlane16_swap(lo, hi, false, false);
    a[j] = r[0];
    b[j] = r[1];
  }
  __builtin_amdgcn_raw_buffer_store_b128(a, srdO, voff, soff, STORE_AUX);
  __builtin_amdgcn_raw_buffer_store_b128(b, srdO, voff, soff + 16 * OROW, STORE_AUX);
}

__global__ __launch_bounds__(512, 2) void gptq_gemm(const char* __restrict__ x, const char* __restrict__ qw,
                                                    const char* __restrict__ sc, const char* __restrict__ zr,
                                                    char* __restrict__ out) {
  __shared__ __attribute__((aligned(1024))) char lds[128 * XROW];
  const unsigned tid = threadIdx.x, lane = tid & 63u;
  const unsigned v = __builtin_amdgcn_readfirstlane(tid >> 6);
  const unsigned w = v & 3u, u = v >> 2;
  const unsigned b = blockIdx.x, xcd = b & 7u, idx = b >> 3;
  const unsigned mt = (xcd >> 1) * 4 + (idx & 3u), nt = (xcd & 1u) * 8 + (idx >> 2);
  const unsigned m0 = mt * 128, f0 = nt * 288;
  const unsigned r = lane & 31u, h = lane >> 5;

  __amdgpu_buffer_rsrc_t srdX = __builtin_amdgcn_make_buffer_rsrc((void*)(x + (size_t)m0 * XROW), (short)0, 128 * XROW, 0x00020000);
  __amdgpu_buffer_rsrc_t srdW = __builtin_amdgcn_make_buffer_rsrc((void*)qw, (short)0, (KK / 8) * WROW, 0x00020000);
  __amdgpu_buffer_rsrc_t srdS = __builtin_amdgcn_make_buffer_rsrc((void*)sc, (short)0, NN * 2, 0x00020000);
  __amdgpu_buffer_rsrc_t srdZ = __builtin_amdgcn_make_buffer_rsrc((void*)zr, (short)0, NN * 2, 0x00020000);
  __amdgpu_buffer_rsrc_t srdO = __builtin_amdgcn_make_buffer_rsrc((void*)(out + (size_t)m0 * OROW), (short)0, 128 * OROW, 0x00020000);

  const unsigned odd = nt & 1u;
  const unsigned ftA = 2 * w + u + odd;
  const unsigned ftS = odd ? 0u : 8u;
  const unsigned pr = 16 * ((r >> 2) & 1u) + (r & 3u) + 4 * (r >> 3);
  const unsigned colA = f0 + 32 * ftA + pr, colS = f0 + 32 * ftS + pr;

  const unsigned xr = 8 * w + (lane >> 3);
  const unsigned voffX = xr * XROW + (((lane & 7u) ^ ((xr >> 1) & 7u)) << 4);
  const unsigned ldsbase = (unsigned)(uintptr_t)(__attribute__((address_space(3))) char*)lds;
  const unsigned m0base = ldsbase + v * 1024;

  auto xround = [&](int t) {
    const int k0 = 2 * t, k1 = 2 * t + 1;
    const unsigned o0 = (k0 / 9) * 32 * XROW + (k0 % 9) * 128, o1 = (k1 / 9) * 32 * XROW + (k1 % 9) * 128;
    const unsigned soff = o0 + u * (o1 - o0);
    asm volatile("s_mov_b32 m0, %0\n\ts_nop 0\n\tbuffer_load_dwordx4 %1, %2, %3 offen lds"
                 ::"s"(m0base + 8192u * t), "v"(voffX), "s"(srdX), "s"(soff) : "memory");
  };
  unsigned wp[36];
  unsigned sraw, zraw;
  const unsigned voffW = h * WROW + colA * 4;
  auto wload = [&](int s, unsigned voff) {
    asm volatile("buffer_load_dword %0, %1, %2, %3 offen" : "=v"(wp[s]) : "v"(voff), "s"(srdW), "s"((unsigned)(s * 2 * WROW)) : "memory");
  };
  asm volatile("s_nop 4\n\tbuffer_load_ushort %0, %2, %3, 0 offen\n\tbuffer_load_ushort %1, %2, %4, 0 offen"
               : "=&v"(sraw), "=&v"(zraw) : "v"(colA * 2), "s"(srdS), "s"(srdZ) : "memory");
#pragma unroll
  for (int g = 0; g < 5; ++g) {
    xround(g);
#pragma unroll
    for (int s = 8 * g; s < 8 * g + 8 && s < 36; ++s) wload(s, voffW);
  }
#pragma unroll
  for (int t = 5; t < 18; ++t) xround(t);

  unsigned rb[4];
#pragma unroll
  for (int q = 0; q < 4; ++q) rb[q] = (r >> 3) * 1024 + (r & 7u) * 128 + (((2 * q + h) ^ ((r >> 1) & 7u)) << 4);
  auto xfrag = [&](unsigned mbase, int s) -> u32x4 {
    return *reinterpret_cast<const u32x4*>(lds + mbase + rb[s & 3] + (s >> 2) * 4096);
  };

  const unsigned voffO = (lane & 15u) * OROW + (lane >> 4) * 16;
  u32x4 wc[36];
  float s24, s20, nzs;
  asm volatile("s_waitcnt vmcnt(54)" : "+v"(sraw), "+v"(zraw)::"memory");
  {
    float sf = __builtin_bit_cast(float, sraw << 16), zf = __builtin_bit_cast(float, zraw << 16);
    s24 = sf * 16777216.f; s20 = sf * 1048576.f; nzs = -zf * sf;
  }
  f32x16 acc = {};
#pragma unroll
  for (int s = 0; s < 36; ++s) {
    const int g = s >> 3;
    const int pos = 2 + (g + 1) + s + 1;
    asm volatile("s_waitcnt vmcnt(%1)" : "+v"(wp[s]) : "i"(56 - pos) : "memory");
    if ((s & 7) == 0) bar();
    wc[s] = dequant(wp[s], s24, s20, nzs);
    acc = mfma(wc[s], xfrag(0, s), acc);
  }
  wait_vm<9>();
  bar();
  epilogue(acc, srdO, voffO, (f0 + 32 * ftA) * 2);
  acc = (f32x16){};
#pragma unroll
  for (int s = 0; s < 36; ++s) {
    if (s == 18) { wait_vm<6>(); bar(); }
    acc = mfma(wc[s], xfrag(36864, s), acc);
  }
  epilogue(acc, srdO, voffO, (f0 + 32 * ftA) * 2 + 32 * OROW);
  acc = (f32x16){};
#pragma unroll
  for (int s = 0; s < 36; ++s) {
    if (s == 18) { wait_vm<4>(); bar(); }
    acc = mfma(wc[s], xfrag(2 * 36864, s), acc);
  }
  epilogue(acc, srdO, voffO, (f0 + 32 * ftA) * 2 + 64 * OROW);
  acc = (f32x16){};
#pragma unroll
  for (int s = 0; s < 36; ++s) acc = mfma(wc[s], xfrag(3 * 36864, s), acc);
  __builtin_amdgcn_sched_barrier(0);
  if (u == 0) {
    const unsigned voffWS = h * WROW + colS * 4;
    asm volatile("buffer_load_ushort %0, %2, %3, 0 offen\n\tbuffer_load_ushort %1, %2, %4, 0 offen"
                 : "=&v"(sraw), "=&v"(zraw) : "v"(colS * 2), "s"(srdS), "s"(srdZ) : "memory");
#pragma unroll
    for (int s = 0; s < 36; ++s) wload(s, voffWS);
    epilogue(acc, srdO, voffO, (f0 + 32 * ftA) * 2 + 96 * OROW);
    asm volatile("s_waitcnt vmcnt(38)" : "+v"(sraw), "+v"(zraw)::"memory");
    {
      float sf = __builtin_bit_cast(float, sraw << 16), zf = __builtin_bit_cast(float, zraw << 16);
      s24 = sf * 16777216.f; s20 = sf * 1048576.f; nzs = -zf * sf;
    }
    acc = (f32x16){};
#pragma unroll
    for (int s = 0; s < 36; ++s) {
      asm volatile("s_waitcnt vmcnt(%1)" : "+v"(wp[s]) : "i"(37 - s) : "memory");
      acc = mfma(dequant(wp[s], s24, s20, nzs), xfrag(w * 36864, s), acc);
    }
    epilogue(acc, srdO, voffO, (f0 + 32 * ftS) * 2 + w * 32 * OROW);
  } else {
    epilogue(acc, srdO, voffO, (f0 + 32 * ftA) * 2 + 96 * OROW);
  }
}

extern "C" void kernel_launch(void* const* d_in, const int* in_sizes, int n_in, void* d_out, int out_size, void* d_ws,
                              size_t ws_size, hipStream_t stream) {
  gptq_gemm<<<256, 512, 0, stream>>>((const char*)d_in[0], (const char*)d_in[1], (const char*)d_in[2], (const char*)d_in[3],
                                     (char*)d_out);
}
```

```cpp
#define STAGE 0
#include <hip/hip_runtime.h>
#include <stdint.h>

typedef __bf16 bf16x8 __attribute__((ext_vector_type(8)));
typedef __bf16 bf16x2 __attribute__((ext_vector_type(2)));
typedef _Float16 f16x2 __attribute__((ext_vector_type(2)));
typedef float f32x16 __attribute__((ext_vector_type(16)));
typedef float f32x2 __attribute__((ext_vector_type(2)));
typedef unsigned u32x4 __attribute__((ext_vector_type(4)));
#define DI __device__ __forceinline__

constexpr int MM = 2048, KK = 576, NN = 4608;
constexpr int XROW = KK * 2;
constexpr int WROW = NN * 4;
constexpr int OROW = NN * 2;

#define STORE_AUX 0

template <int N> DI void wait_vm() { asm volatile("s_waitcnt vmcnt(%0)" ::"i"(N) : "memory"); }
DI void bar() { asm volatile("s_barrier" ::: "memory"); }

DI unsigned pack_bf16(float a, float b) {
  f32x2 v = {a, b};
  bf16x2 r = __builtin_convertvector(v, bf16x2);
  return __builtin_bit_cast(unsigned, r);
}

DI u32x4 dequant(unsigned p, float s24, float s20, float nzs) {
  unsigned p8 = p >> 8;
  f16x2 m0 = __builtin_bit_cast(f16x2, p & 0x000F000Fu);
  f16x2 m1 = __builtin_bit_cast(f16x2, p & 0x00F000F0u);
  f16x2 m2 = __builtin_bit_cast(f16x2, p8 & 0x000F000Fu);
  f16x2 m3 = __builtin_bit_cast(f16x2, p8 & 0x00F000F0u);
  u32x4 r;
  r[0] = pack_bf16(__builtin_fmaf((float)m0[0], s24, nzs), __builtin_fmaf((float)m1[0], s20, nzs));
  r[1] = pack_bf16(__builtin_fmaf((float)m2[0], s24, nzs), __builtin_fmaf((float)m3[0], s20, nzs));
  r[2] = pack_bf16(__builtin_fmaf((float)m0[1], s24, nzs), __builtin_fmaf((float)m1[1], s20, nzs));
  r[3] = pack_bf16(__builtin_fmaf((float)m2[1], s24, nzs), __builtin_fmaf((float)m3[1], s20, nzs));
  return r;
}

DI f32x16 mfma(u32x4 a, u32x4 b, f32x16 c) {
  return __builtin_amdgcn_mfma_f32_32x32x16_bf16(__builtin_bit_cast(bf16x8, a), __builtin_bit_cast(bf16x8, b), c, 0, 0, 0);
}

DI void epilogue(const f32x16& acc, __amdgpu_buffer_rsrc_t srdO, unsigned voff, unsigned soff) {
  u32x4 a, b;
#pragma unroll
  for (int j = 0; j < 4; ++j) {
    unsigned lo = pack_bf16(acc[2 * j], acc[2 * j + 1]);
    unsigned hi = pack_bf16(acc[8 + 2 * j], acc[8 + 2 * j + 1]);
    auto r = __builtin_amdgcn_permlane16_swap(lo, hi, false, false);
    a[j] = r[0];
    b[j] = r[1];
  }
  __builtin_amdgcn_raw_buffer_store_b128(a, srdO, voff, soff, STORE_AUX);
  __builtin_amdgcn_raw_buffer_store_b128(b, srdO, voff, soff + 16 * OROW, STORE_AUX);
}

__global__ __launch_bounds__(512, 2) void gptq_gemm(const char* __restrict__ x, const char* __restrict__ qw,
                                                    const char* __restrict__ sc, const char* __restrict__ zr,
                                                    char* __restrict__ out, unsigned long long* __restrict__ ws) {
  unsigned long long st[12];
  st[0] = __builtin_amdgcn_s_memrealtime(); st[1] = __builtin_amdgcn_s_memrealtime();
  __shared__ __attribute__((aligned(1024))) char lds[128 * XROW];
  const unsigned tid = threadIdx.x, lane = tid & 63u;
  const unsigned v = __builtin_amdgcn_readfirstlane(tid >> 6);
  const unsigned w = v & 3u, u = v >> 2;
  const unsigned b = blockIdx.x, xcd = b & 7u, idx = b >> 3;
  const unsigned mt = (xcd >> 1) * 4 + (idx & 3u), nt = (xcd & 1u) * 8 + (idx >> 2);
  const unsigned m0 = mt * 128, f0 = nt * 288;
  const unsigned r = lane & 31u, h = lane >> 5;

  __amdgpu_buffer_rsrc_t srdX = __builtin_amdgcn_make_buffer_rsrc((void*)(x + (size_t)m0 * XROW), (short)0, 128 * XROW, 0x00020000);
  __amdgpu_buffer_rsrc_t srdW = __builtin_amdgcn_make_buffer_rsrc((void*)qw, (short)0, (KK / 8) * WROW, 0x00020000);
  __amdgpu_buffer_rsrc_t srdS = __builtin_amdgcn_make_buffer_rsrc((void*)sc, (short)0, NN * 2, 0x00020000);
  __amdgpu_buffer_rsrc_t srdZ = __builtin_amdgcn_make_buffer_rsrc((void*)zr, (short)0, NN * 2, 0x00020000);
  __amdgpu_buffer_rsrc_t srdO = __builtin_amdgcn_make_buffer_rsrc((void*)(out + (size_t)m0 * OROW), (short)0, 128 * OROW, 0x00020000);

  const unsigned odd = nt & 1u;
  const unsigned ftA = 2 * w + u + odd;
  const unsigned ftS = odd ? 0u : 8u;
  const unsigned pr = 16 * ((r >> 2) & 1u) + (r & 3u) + 4 * (r >> 3);
  const unsigned colA = f0 + 32 * ftA + pr, colS = f0 + 32 * ftS + pr;

  const unsigned xr = 8 * w + (lane >> 3);
  const unsigned voffX = xr * XROW + (((lane & 7u) ^ ((xr >> 1) & 7u)) << 4);
  const unsigned ldsbase = (unsigned)(uintptr_t)(__attribute__((address_space(3))) char*)lds;
  const unsigned m0base = ldsbase + v * 1024;

  auto xround = [&](int t) {
    const int k0 = 2 * t, k1 = 2 * t + 1;
    const unsigned o0 = (k0 / 9) * 32 * XROW + (k0 % 9) * 128, o1 = (k1 / 9) * 32 * XROW + (k1 % 9) * 128;
    const unsigned soff = o0 + u * (o1 - o0);
    asm volatile("s_mov_b32 m0, %0\n\ts_nop 0\n\tbuffer_load_dwordx4 %1, %2, %3 offen lds"
                 ::"s"(m0base + 8192u * t), "v"(voffX), "s"(srdX), "s"(soff) : "memory");
  };
  unsigned wp[36];
  unsigned sraw, zraw;
  const unsigned voffW = h * WROW + colA * 4;
  auto wload = [&](int s, unsigned voff) {
    asm volatile("buffer_load_dword %0, %1, %2, %3 offen" : "=v"(wp[s]) : "v"(voff), "s"(srdW), "s"((unsigned)(s * 2 * WROW)) : "memory");
  };
  asm volatile("s_nop 4\n\tbuffer_load_ushort %0, %2, %3, 0 offen\n\tbuffer_load_ushort %1, %2, %4, 0 offen"
               : "=&v"(sraw), "=&v"(zraw) : "v"(colA * 2), "s"(srdS), "s"(srdZ) : "memory");
#pragma unroll
  for (int g = 0; g < 5; ++g) {
    xround(g);
#pragma unroll
    for (int s = 8 * g; s < 8 * g + 8 && s < 36; ++s) wload(s, voffW);
  }
#pragma unroll
  for (int t = 5; t < 18; ++t) xround(t);

  __builtin_amdgcn_sched_barrier(0); st[2] = __builtin_amdgcn_s_memrealtime(); __builtin_amdgcn_sched_barrier(0);
  unsigned rb[4];
#pragma unroll
  for (int q = 0; q < 4; ++q) rb[q] = (r >> 3) * 1024 + (r & 7u) * 128 + (((2 * q + h) ^ ((r >> 1) & 7u)) << 4);
  auto xfrag = [&](unsigned mbase, int s) -> u32x4 {
    return *reinterpret_cast<const u32x4*>(lds + mbase + rb[s & 3] + (s >> 2) * 4096);
  };

  const unsigned voffO = (lane & 15u) * OROW + (lane >> 4) * 16;
  u32x4 wc[36];
  float s24, s20, nzs;
  asm volatile("s_waitcnt vmcnt(54)" : "+v"(sraw), "+v"(zraw)::"memory");
  {
    float sf = __builtin_bit_cast(float, sraw << 16), zf = __builtin_bit_cast(float, zraw << 16);
    s24 = sf * 16777216.f; s20 = sf * 1048576.f; nzs = -zf * sf;
  }
  f32x16 acc = {};
#pragma unroll
  for (int s = 0; s < 36; ++s) {
    const int g = s >> 3;
    const int pos = 2 + (g + 1) + s + 1;
    asm volatile("s_waitcnt vmcnt(%1)" : "+v"(wp[s]) : "i"(56 - pos) : "memory");
    if ((s & 7) == 0) bar();
    if (s == 0) { __builtin_amdgcn_sched_barrier(0); st[3] = __builtin_amdgcn_s_memrealtime(); __builtin_amdgcn_sched_barrier(0);}
    if (s == 32) { __builtin_amdgcn_sched_barrier(0); st[4] = __builtin_amdgcn_s_memrealtime(); __builtin_amdgcn_sched_barrier(0);}
    wc[s] = dequant(wp[s], s24, s20, nzs);
    acc = mfma(wc[s], xfrag(0, s), acc);
  }
  wait_vm<9>();
  bar();
  __builtin_amdgcn_sched_barrier(0); st[5] = __builtin_amdgcn_s_memrealtime(); __builtin_amdgcn_sched_barrier(0);
  epilogue(acc, srdO, voffO, (f0 + 32 * ftA) * 2);
  acc = (f32x16){};
#pragma unroll
  for (int s = 0; s < 36; ++s) {
    if (s == 18) { wait_vm<6>(); bar(); }
    acc = mfma(wc[s], xfrag(36864, s), acc);
  }
  epilogue(acc, srdO, voffO, (f0 + 32 * ftA) * 2 + 32 * OROW);
  __builtin_amdgcn_sched_barrier(0); st[6] = __builtin_amdgcn_s_memrealtime(); __builtin_amdgcn_sched_barrier(0);
  acc = (f32x16){};
#pragma unroll
  for (int s = 0; s < 36; ++s) {
    if (s == 18) { wait_vm<4>(); bar(); }
    acc = mfma(wc[s], xfrag(2 * 36864, s), acc);
  }
  epilogue(acc, srdO, voffO, (f0 + 32 * ftA) * 2 + 64 * OROW);
  __builtin_amdgcn_sched_barrier(0); st[7] = __builtin_amdgcn_s_memrealtime(); __builtin_amdgcn_sched_barrier(0);
  acc = (f32x16){};
#pragma unroll
  for (int s = 0; s < 36; ++s) acc = mfma(wc[s], xfrag(3 * 36864, s), acc);
  __builtin_amdgcn_sched_barrier(0); st[8] = __builtin_amdgcn_s_memrealtime(); __builtin_amdgcn_sched_barrier(0);
  if (u == 0) {
    const unsigned voffWS = h * WROW + colS * 4;
    asm volatile("buffer_load_ushort %0, %2, %3, 0 offen\n\tbuffer_load_ushort %1, %2, %4, 0 offen"
                 : "=&v"(sraw), "=&v"(zraw) : "v"(colS * 2), "s"(srdS), "s"(srdZ) : "memory");
#pragma unroll
    for (int s = 0; s < 36; ++s) wload(s, voffWS);
    epilogue(acc, srdO, voffO, (f0 + 32 * ftA) * 2 + 96 * OROW);
    asm volatile("s_waitcnt vmcnt(38)" : "+v"(sraw), "+v"(zraw)::"memory");
    {
      float sf = __builtin_bit_cast(float, sraw << 16), zf = __builtin_bit_cast(float, zraw << 16);
      s24 = sf * 16777216.f; s20 = sf * 1048576.f; nzs = -zf * sf;
    }
    acc = (f32x16){};
#pragma unroll
    for (int s = 0; s < 36; ++s) {
      asm volatile("s_waitcnt vmcnt(%1)" : "+v"(wp[s]) : "i"(37 - s) : "memory");
      acc = mfma(dequant(wp[s], s24, s20, nzs), xfrag(w * 36864, s), acc);
    }
    epilogue(acc, srdO, voffO, (f0 + 32 * ftS) * 2 + w * 32 * OROW);
  } else {
    epilogue(acc, srdO, voffO, (f0 + 32 * ftA) * 2 + 96 * OROW);
  }
  __builtin_amdgcn_sched_barrier(0); st[9] = __builtin_amdgcn_s_memrealtime(); __builtin_amdgcn_sched_barrier(0);
  asm volatile("s_waitcnt vmcnt(0)" ::: "memory");
  st[10] = __builtin_amdgcn_s_memrealtime();
  st[11] = __builtin_amdgcn_s_memrealtime();
  { unsigned long long tend = st[STAGE] + 4000ull; while (__builtin_amdgcn_s_memrealtime() < tend) __builtin_amdgcn_s_sleep(2); }
}

extern "C" void kernel_launch(void* const* d_in, const int* in_sizes, int n_in, void* d_out, int out_size, void* d_ws,
                              size_t ws_size, hipStream_t stream) {
  gptq_gemm<<<256, 512, 0, stream>>>((const char*)d_in[0], (const char*)d_in[1], (const char*)d_in[2], (const char*)d_in[3],
                                     (char*)d_out, (unsigned long long*)d_ws);
}
```

```cpp
#include <hip/hip_runtime.h>
#include <stdint.h>

__global__ __launch_bounds__(512, 2) void gptq_gemm(const void* x, unsigned xn, unsigned xf, const void* qw, unsigned wn, unsigned wf, void* out, unsigned on, unsigned of,
                                                    const void* sc, const void* zr) {
  __shared__ __attribute__((aligned(1024))) char lds[160256];
  const unsigned ldsbase = (unsigned)(uintptr_t)(__attribute__((address_space(3))) char*)lds;
  asm volatile(
      "s_load_dwordx16 s[8:23], %0, 0\n"
      "s_setprio 3\n"
      "v_lshrrev_b32 v105, 6, %2\n"
      "v_bfe_u32 v108, %2, 4, 2\n"
      "v_readfirstlane_b32 s28, v105\n"
      "s_lshr_b32 s56, %1, 5\n"
      "s_and_b32 s64, %1, 1\n"
      "v_and_b32 v107, 15, %2\n"
      "s_lshl3_add_u32 s64, s64, s56\n"
      "v_mul_u32_u24 v23, 18432, v108\n"
      "s_mul_i32 s64, s64, 9\n"
      "v_lshl_add_u32 v23, v107, 3, v23\n"
      "s_and_b32 s57, s56, 1\n"
      "v_lshrrev_b32 v105, 3, %2\n"
      "s_add_u32 s63, s64, s57\n"
      "v_mul_u32_u24 v1, 1152, v105\n"
      "s_and_b32 s65, s28, 3\n"
      "s_lshr_b32 s59, s28, 2\n"
      "s_lshl1_add_u32 s63, s65, s63\n"
      "s_add_u32 s63, s63, s59\n"
      "v_lshrrev_b32 v105, 4, %2\n"
      "s_lshl_b32 s66, s63, 7\n"
      "v_xor_b32 v105, v105, %2\n"
      "s_bfe_u32 s30, %1, 0x20003\n"
      "v_add_u32 v2, s66, v23\n"
      "s_bfe_u32 s60, %1, 0x20001\n"
      "v_add_u32 v21, 73728, v2\n"
      "s_lshl2_add_u32 s60, s60, s30\n"
      "v_and_b32 v105, 7, v105\n"
      "s_mul_i32 s29, s28, 18432\n"
      "v_lshl_add_u32 v1, v105, 4, v1\n"
      "s_add_u32 m0, s29, %3\n"
      "s_mul_i32 s66, s60, 147456\n"
      "s_waitcnt lgkmcnt(0)\n"
      "buffer_load_dwordx2 v[114:115], v2, s[12:15], 0 offen \n"
      "buffer_load_dwordx2 v[122:123], v21, s[12:15], 0 offen \n"
      "buffer_load_dwordx4 v1, s[8:11], s66 offen  lds\n"
      "s_sub_u32 s66, s66, 4096\n"
      "s_ashr_i32 s67, s66, 31\n"
      "s_add_u32 s8, s8, s66\n"
      "s_addc_u32 s9, s9, s67\n"
      "s_add_u32 s29, s29, %3\n"
      "s_xor_b32 s57, s57, 1\n"
      "s_lshl3_add_u32 s64, s57, s64\n"
      "s_mul_i32 s59, s59, 73728\n"
      "s_lshl1_add_u32 s59, s65, s59\n"
      "s_lshl_b32 s66, s64, 7\n"
      "s_add_u32 s59, s59, s66\n"
      "v_add_u32 v23, s59, v23\n"
      "v_and_b32 v104, 63, %2\n"
      "s_mov_b32 s32, 0\n"
      "s_mov_b32 s42, 76800\n"
      "s_mov_b32 s33, 147456\n"
      "s_mov_b32 s43, 2176\n"
      "s_mov_b32 s44, 77952\n"
      "s_getpc_b64 s[24:25]\n"
      ".Lpc%=:\n"
      "s_and_b32 s66, %1, -8\n"
      "s_add_u32 s66, s66, s28\n"
      "s_lshl_b32 s66, s66, 6\n"
      "s_min_u32 s66, s66, .Lend%=-.Lpc%=-4\n"
      "s_load_dword s31, s[24:25], s66\n"
      "s_barrier\n"
      "s_setprio 2\n"
      "buffer_load_ushort v18, v23, s[12:15], s32 offen\n"
      "buffer_load_dwordx2 v[130:131], v2, s[12:15], s33 offen \n"
      "buffer_load_dwordx2 v[138:139], v21, s[12:15], s33 offen \n"
      "buffer_load_dwordx4 v1, s[8:11], s43 offen offset:2048  lds\n"
      "s_setprio 0\n"
      "s_and_b32 s58, s28, 1\n"
      "s_add_u32 s58, s58, s60\n"
      "s_mul_i32 s58, s58, 1179648\n"
      "s_min_u32 s58, s58, 18874364\n"
      "s_load_dword s31, s[16:17], s58\n"
      "s_mov_b32 s34, 294912\n"
      "s_mov_b32 s45, 3328\n"
      "s_mov_b32 s46, 76032\n"
      "s_mov_b32 s35, 442368\n"
      "s_mov_b32 s47, 1408\n"
      "s_mov_b32 s48, 78208\n"
      "s_mov_b32 s36, 589824\n"
      "s_mov_b32 s49, 3584\n"
      "s_mov_b32 s50, 76288\n"
      "s_mov_b32 s37, 737280\n"
      "s_mov_b32 s51, 1664\n"
      "s_mov_b32 s52, 78464\n"
      "s_mov_b32 s38, 884736\n"
      "s_mov_b32 s53, 3840\n"
      "s_mov_b32 s54, 76544\n"
      "s_mov_b32 s39, 1032192\n"
      "s_mov_b32 s55, 1920\n"
      "s_mov_b32 s56, 78720\n"
      "s_mov_b32 s40, 1179648\n"
      "s_mov_b32 s57, 4096\n"
      "s_mov_b32 s58, 76800\n"
      "s_mov_b32 s61, 0x8008\n"
      "s_mov_b32 s62, 0x780078\n"
      "v_mov_b32 v10, 0x41804180\n"
      "s_lshl_b32 s60, s60, 7\n"
      "v_add_u32 v3, s60, v107\n"
      "v_mul_u32_u24 v3, 9216, v3\n"
      "v_lshl_add_u32 v3, v108, 4, v3\n"
      "v_lshrrev_b32 v105, 1, v107\n"
      "v_xor_b32 v105, v105, v108\n"
      "v_lshrrev_b32 v106, 3, v107\n"
      "v_mul_u32_u24 v106, 17408, v106\n"
      "v_lshl_add_u32 v4, v107, 7, v106\n"
      "v_lshl_add_u32 v4, v105, 4, v4\n"
      "v_add_u32 v4, %3, v4\n"
      "v_xor_b32 v5, 64, v4\n"
      "v_add_u32 v6, 73728, v4\n"
      "v_add_u32 v7, 73728, v5\n"
      "s_mul_i32 s66, s65, 36864\n"
      "s_lshr_b32 s59, s28, 2\n"
      "s_lshl_b32 s59, s59, 10\n"
      "s_add_u32 s66, s66, s59\n"
      "v_add_u32 v8, s66, v4\n"
      "v_add_u32 v9, s66, v5\n"
      "s_add_u32 s66, %3, 147456\n"
      "v_lshl_add_u32 v13, v104, 4, s66\n"
      "s_lshr_b32 s59, s28, 1\n"
      "s_and_b32 s60, s28, 1\n"
      "s_lshl_b32 s59, s59, 10\n"
      "s_lshl3_add_u32 s59, s60, s59\n"
      "v_add_u32 v14, s59, v13\n"
      "s_add_u32 s66, %3, 159744\n"
      "v_lshl_add_u32 v22, v107, 5, s66\n"
      "s_lshl_b32 s68, s28, 2\n"
      "s_lshl_b32 s63, s63, 6\n"
      "s_lshl_b32 s64, s64, 6\n"
      "s_sub_u32 s59, s64, s63\n"
      "v_bfe_i32 v105, v104, 3, 1\n"
      "v_and_b32 v105, s59, v105\n"
      "v_and_b32 v106, 7, v104\n"
      "v_lshl_add_u32 v106, v108, 3, v106\n"
      "v_lshl_add_u32 v106, v106, 1, s63\n"
      "v_add_u32 v11, v105, v106\n"
      "s_mul_i32 s59, s28, 147456\n"
      "s_add_u32 s64, s64, s59\n"
      "v_bfe_i32 v105, v104, 0, 1\n"
      "v_and_b32 v105, 0xaaaaaaaa, v105\n"
      "v_or_b32 v15, 0x44444444, v105\n"
      "v_mov_b32 v100, 0x3f803f80\n"
      "v_mov_b32 v96, 0\n"
      "v_mov_b32 v101, 0x3f803f80\n"
      "v_mov_b32 v97, 0\n"
      "v_mov_b32 v102, 0x3f803f80\n"
      "v_mov_b32 v98, 0\n"
      "v_mov_b32 v103, 0x3f803f80\n"
      "v_mov_b32 v99, 0\n"
      "buffer_load_dwordx4 v1, s[8:11], s42 offen offset:1024  lds\n"
      "buffer_load_ushort v19, v23, s[12:15], s33 offen\n"
      "s_cmp_lt_u32 s28, 4\n"
      "s_cbranch_scc1 .Lpa%=\n"
      "s_setprio 1\n"
      ".Lpa%=:\n"
      "s_waitcnt vmcnt(8)\n"
      "v_mul_u32_u24_sdwa v116, v115, s61 dst_sel:DWORD dst_unused:UNUSED_PAD src0_sel:BYTE_0 src1_sel:DWORD\n"
      "v_mul_u32_u24_sdwa v117, v115, s61 dst_sel:DWORD dst_unused:UNUSED_PAD src0_sel:BYTE_1 src1_sel:DWORD\n"
      "v_mul_u32_u24_sdwa v118, v115, s61 dst_sel:DWORD dst_unused:UNUSED_PAD src0_sel:BYTE_2 src1_sel:DWORD\n"
      "v_mul_u32_u24_sdwa v119, v115, s61 dst_sel:DWORD dst_unused:UNUSED_PAD src0_sel:BYTE_3 src1_sel:DWORD\n"
      "v_mul_u32_u24_sdwa v112, v114, s61 dst_sel:DWORD dst_unused:UNUSED_PAD src0_sel:BYTE_0 src1_sel:DWORD\n"
      "v_mul_u32_u24_sdwa v113, v114, s61 dst_sel:DWORD dst_unused:UNUSED_PAD src0_sel:BYTE_1 src1_sel:DWORD\n"
      "v_mul_u32_u24_sdwa v115, v114, s61 dst_sel:DWORD dst_unused:UNUSED_PAD src0_sel:BYTE_3 src1_sel:DWORD\n"
      "v_mul_u32_u24_sdwa v114, v114, s61 dst_sel:DWORD dst_unused:UNUSED_PAD src0_sel:BYTE_2 src1_sel:DWORD\n"
      "v_and_or_b32 v112, v112, s62, v10\n"
      "v_and_or_b32 v113, v113, s62, v10\n"
      "v_and_or_b32 v115, v115, s62, v10\n"
      "v_and_or_b32 v114, v114, s62, v10\n"
      "v_and_or_b32 v116, v116, s62, v10\n"
      "v_and_or_b32 v117, v117, s62, v10\n"
      "v_and_or_b32 v118, v118, s62, v10\n"
      "v_and_or_b32 v119, v119, s62, v10\n"
      "s_waitcnt vmcnt(6)\n"
      "s_barrier\n"
      "ds_read_b128 v[24:27], v4 offset:0\n"
      "ds_read_b128 v[28:31], v4 offset:36864\n"
      "ds_read_b128 v[32:35], v6 offset:0\n"
      "ds_read_b128 v[36:39], v6 offset:36864\n"
      "v_mul_u32_u24_sdwa v124, v123, s61 dst_sel:DWORD dst_unused:UNUSED_PAD src0_sel:BYTE_0 src1_sel:DWORD\n"
      "v_mul_u32_u24_sdwa v125, v123, s61 dst_sel:DWORD dst_unused:UNUSED_PAD src0_sel:BYTE_1 src1_sel:DWORD\n"
      "v_mul_u32_u24_sdwa v126, v123, s61 dst_sel:DWORD dst_unused:UNUSED_PAD src0_sel:BYTE_2 src1_sel:DWORD\n"
      "v_mul_u32_u24_sdwa v127, v123, s61 dst_sel:DWORD dst_unused:UNUSED_PAD src0_sel:BYTE_3 src1_sel:DWORD\n"
      "v_mul_u32_u24_sdwa v120, v122, s61 dst_sel:DWORD dst_unused:UNUSED_PAD src0_sel:BYTE_0 src1_sel:DWORD\n"
      "v_mul_u32_u24_sdwa v121, v122, s61 dst_sel:DWORD dst_unused:UNUSED_PAD src0_sel:BYTE_1 src1_sel:DWORD\n"
      "v_mul_u32_u24_sdwa v123, v122, s61 dst_sel:DWORD dst_unused:UNUSED_PAD src0_sel:BYTE_3 src1_sel:DWORD\n"
      "v_mul_u32_u24_sdwa v122, v122, s61 dst_sel:DWORD dst_unused:UNUSED_PAD src0_sel:BYTE_2 src1_sel:DWORD\n"
      "s_waitcnt lgkmcnt(3)\n"
      "v_mfma_f32_16x16x32_bf16 v[48:51], v[112:115], v[24:27], 0\n"
      "buffer_load_dwordx2 v[146:147], v2, s[12:15], s34 offen \n"
      "ds_read_b128 v[40:43], v5 offset:0\n"
      "v_and_or_b32 v120, v120, s62, v10\n"
      "v_mfma_f32_16x16x32_bf16 v[52:55], v[116:119], v[24:27], 0\n"
      "s_waitcnt lgkmcnt(3)\n"
      "buffer_load_dwordx2 v[154:155], v21, s[12:15], s34 offen \n"
      "s_add_u32 m0, s29, 3072\n"
      "v_and_or_b32 v121, v121, s62, v10\n"
      "v_mfma_f32_16x16x32_bf16 v[56:59], v[112:115], v[28:31], 0\n"
      "buffer_load_dwordx4 v1, s[8:11], s45 offen offset:1024  lds\n"
      "v_and_or_b32 v123, v123, s62, v10\n"
      "v_and_or_b32 v122, v122, s62, v10\n"
      "v_mfma_f32_16x16x32_bf16 v[60:63], v[116:119], v[28:31], 0\n"
      "s_waitcnt vmcnt(8) lgkmcnt(1)\n"
      "ds_read_b128 v[44:47], v5 offset:36864\n"
      "buffer_load_dwordx4 v1, s[8:11], s44 offen offset:0  lds\n"
      "v_mul_u32_u24_sdwa v16, v18, s61 dst_sel:DWORD dst_unused:UNUSED_PAD src0_sel:BYTE_0 src1_sel:DWORD\n"
      "v_mul_u32_u24_sdwa v17, v18, s61 dst_sel:DWORD dst_unused:UNUSED_PAD src0_sel:BYTE_1 src1_sel:DWORD\n"
      "v_mfma_f32_16x16x32_bf16 v[64:67], v[112:115], v[32:35], 0\n"
      "buffer_load_ushort v20, v23, s[12:15], s34 offen\n"
      "v_and_or_b32 v16, v16, s62, v10\n"
      "v_and_or_b32 v17, v17, s62, v10\n"
      "v_mfma_f32_16x16x32_bf16 v[68:71], v[116:119], v[32:35], 0\n"
      "ds_write_b64 v14, v[16:17] offset:0\n"
      "ds_read_b128 v[104:107], v7 offset:0\n"
      "global_load_ushort v12, v11, s[22:23]\n"
      "v_and_or_b32 v124, v124, s62, v10\n"
      "v_and_or_b32 v125, v125, s62, v10\n"
      "v_mfma_f32_16x16x32_bf16 v[72:75], v[112:115], v[36:39], 0\n"
      "global_load_ushort v11, v11, s[20:21]\n"
      "v_and_or_b32 v126, v126, s62, v10\n"
      "v_and_or_b32 v127, v127, s62, v10\n"
      "v_mfma_f32_16x16x32_bf16 v[76:79], v[116:119], v[36:39], 0\n"
      "s_waitcnt vmcnt(11) lgkmcnt(2)\n"
      "ds_read_b128 v[108:111], v7 offset:36864\n"
      "buffer_load_dwordx2 v[162:163], v2, s[12:15], s35 offen \n"
      "v_mul_u32_u24_sdwa v132, v131, s61 dst_sel:DWORD dst_unused:UNUSED_PAD src0_sel:BYTE_0 src1_sel:DWORD\n"
      "v_mul_u32_u24_sdwa v133, v131, s61 dst_sel:DWORD dst_unused:UNUSED_PAD src0_sel:BYTE_1 src1_sel:DWORD\n"
      "v_mfma_f32_16x16x32_bf16 v[48:51], v[120:123], v[40:43], v[48:51]\n"
      "buffer_load_dwordx2 v[170:171], v21, s[12:15], s35 offen \n"
      "v_mul_u32_u24_sdwa v134, v131, s61 dst_sel:DWORD dst_unused:UNUSED_PAD src0_sel:BYTE_2 src1_sel:DWORD\n"
      "v_mul_u32_u24_sdwa v135, v131, s61 dst_sel:DWORD dst_unused:UNUSED_PAD src0_sel:BYTE_3 src1_sel:DWORD\n"
      "v_mfma_f32_16x16x32_bf16 v[52:55], v[124:127], v[40:43], v[52:55]\n"
      "s_waitcnt vmcnt(10) lgkmcnt(1)\n"
      "s_barrier\n"
      "ds_read_b128 v[24:27], v4 offset:2048\n"
      "buffer_load_dwordx4 v1, s[8:11], s46 offen offset:2048  lds\n"
      "v_mul_u32_u24_sdwa v128, v130, s61 dst_sel:DWORD dst_unused:UNUSED_PAD src0_sel:BYTE_0 src1_sel:DWORD\n"
      "v_mul_u32_u24_sdwa v129, v130, s61 dst_sel:DWORD dst_unused:UNUSED_PAD src0_sel:BYTE_1 src1_sel:DWORD\n"
      "v_mfma_f32_16x16x32_bf16 v[56:59], v[120:123], v[44:47], v[56:59]\n"
      "ds_read_b128 v[28:31], v4 offset:38912\n"
      "v_mul_u32_u24_sdwa v131, v130, s61 dst_sel:DWORD dst_unused:UNUSED_PAD src0_sel:BYTE_3 src1_sel:DWORD\n"
      "v_mul_u32_u24_sdwa v130, v130, s61 dst_sel:DWORD dst_unused:UNUSED_PAD src0_sel:BYTE_2 src1_sel:DWORD\n"
      "v_mfma_f32_16x16x32_bf16 v[60:63], v[124:127], v[44:47], v[60:63]\n"
      "ds_read_b128 v[88:91], v8 offset:0\n"
      "v_and_or_b32 v128, v128, s62, v10\n"
      "v_and_or_b32 v129, v129, s62, v10\n"
      "v_mfma_f32_16x16x32_bf16 v[64:67], v[120:123], v[104:107], v[64:67]\n"
      "ds_read_b128 v[32:35], v13 offset:0\n"
      "v_and_or_b32 v131, v131, s62, v10\n"
      "v_and_or_b32 v130, v130, s62, v10\n"
      "v_mfma_f32_16x16x32_bf16 v[68:71], v[124:127], v[104:107], v[68:71]\n"
      "s_waitcnt lgkmcnt(3)\n"
      "v_and_or_b32 v132, v132, s62, v10\n"
      "v_and_or_b32 v133, v133, s62, v10\n"
      "v_mfma_f32_16x16x32_bf16 v[72:75], v[120:123], v[108:111], v[72:75]\n"
      "ds_read_b128 v[36:39], v13 offset:1024\n"
      "v_and_or_b32 v134, v134, s62, v10\n"
      "v_and_or_b32 v135, v135, s62, v10\n"
      "v_mfma_f32_16x16x32_bf16 v[76:79], v[124:127], v[108:111], v[76:79]\n"
      "ds_read_b128 v[92:95], v9 offset:0\n"
      "ds_read_b128 v[40:43], v13 offset:2048\n"
      "ds_read_b128 v[44:47], v13 offset:3072\n"
      "v_mfma_f32_16x16x32_bf16 v[48:51], v[128:131], v[24:27], v[48:51]\n"
      "ds_read_b128 v[104:107], v6 offset:2048\n"
      "buffer_load_dwordx4 v1, s[8:11], s47 offen offset:3072  lds\n"
      "v_mul_u32_u24_sdwa v140, v139, s61 dst_sel:DWORD dst_unused:UNUSED_PAD src0_sel:BYTE_0 src1_sel:DWORD\n"
      "v_mfma_f32_16x16x32_bf16 v[52:55], v[132:135], v[24:27], v[52:55]\n"
      "s_waitcnt lgkmcnt(5)\n"
      "v_mul_u32_u24_sdwa v141, v139, s61 dst_sel:DWORD dst_unused:UNUSED_PAD src0_sel:BYTE_1 src1_sel:DWORD\n"
      "v_mul_u32_u24_sdwa v142, v139, s61 dst_sel:DWORD dst_unused:UNUSED_PAD src0_sel:BYTE_2 src1_sel:DWORD\n"
      "v_mfma_f32_16x16x32_bf16 v[56:59], v[128:131], v[28:31], v[56:59]\n"
      "ds_read_b128 v[108:111], v6 offset:38912\n"
      "v_mul_u32_u24_sdwa v143, v139, s61 dst_sel:DWORD dst_unused:UNUSED_PAD src0_sel:BYTE_3 src1_sel:DWORD\n"
      "v_mul_u32_u24_sdwa v136, v138, s61 dst_sel:DWORD dst_unused:UNUSED_PAD src0_sel:BYTE_0 src1_sel:DWORD\n"
      "v_mfma_f32_16x16x32_bf16 v[60:63], v[132:135], v[28:31], v[60:63]\n"
      "ds_read_b128 v[24:27], v5 offset:2048\n"
      "v_mul_u32_u24_sdwa v137, v138, s61 dst_sel:DWORD dst_unused:UNUSED_PAD src0_sel:BYTE_1 src1_sel:DWORD\n"
      "v_mul_u32_u24_sdwa v139, v138, s61 dst_sel:DWORD dst_unused:UNUSED_PAD src0_sel:BYTE_3 src1_sel:DWORD\n"
      "v_mfma_f32_16x16x32_bf16 v[80:83], v[32:35], v[88:91], 0\n"
      "s_waitcnt lgkmcnt(2)\n"
      "buffer_load_ushort v18, v23, s[12:15], s35 offen\n"
      "v_mul_u32_u24_sdwa v138, v138, s61 dst_sel:DWORD dst_unused:UNUSED_PAD src0_sel:BYTE_2 src1_sel:DWORD\n"
      "v_mfma_f32_16x16x32_bf16 v[84:87], v[36:39], v[88:91], 0\n"
      "ds_read_b128 v[28:31], v5 offset:38912\n"
      "v_and_or_b32 v136, v136, s62, v10\n"
      "v_and_or_b32 v137, v137, s62, v10\n"
      "v_mfma_f32_16x16x32_bf16 v[80:83], v[40:43], v[92:95], v[80:83]\n"
      "ds_read_b128 v[32:35], v7 offset:2048\n"
      "v_and_or_b32 v139, v139, s62, v10\n"
      "v_and_or_b32 v138, v138, s62, v10\n"
      "v_mfma_f32_16x16x32_bf16 v[84:87], v[44:47], v[92:95], v[84:87]\n"
      "s_waitcnt vmcnt(12) lgkmcnt(2)\n"
      "v_mul_u32_u24_sdwa v16, v19, s61 dst_sel:DWORD dst_unused:UNUSED_PAD src0_sel:BYTE_0 src1_sel:DWORD\n"
      "v_mul_u32_u24_sdwa v17, v19, s61 dst_sel:DWORD dst_unused:UNUSED_PAD src0_sel:BYTE_1 src1_sel:DWORD\n"
      "v_smfmac_f32_16x16x64_bf16 v[96:99], v[100:103], v[88:95], v15\n"
      "buffer_load_dwordx2 v[178:179], v2, s[12:15], s36 offen \n"
      "v_and_or_b32 v16, v16, s62, v10\n"
      "v_and_or_b32 v17, v17, s62, v10\n"
      "v_mfma_f32_16x16x32_bf16 v[64:67], v[128:131], v[104:107], v[64:67]\n"
      "ds_write_b64 v14, v[16:17] offset:4096\n"
      "v_and_or_b32 v140, v140, s62, v10\n"
      "v_and_or_b32 v141, v141, s62, v10\n"
      "v_mfma_f32_16x16x32_bf16 v[68:71], v[132:135], v[104:107], v[68:71]\n"
      "ds_read_b128 v[36:39], v7 offset:38912\n"
      "v_and_or_b32 v142, v142, s62, v10\n"
      "v_and_or_b32 v143, v143, s62, v10\n"
      "v_mfma_f32_16x16x32_bf16 v[72:75], v[128:131], v[108:111], v[72:75]\n"
      "s_waitcnt vmcnt(12) lgkmcnt(2)\n"
      "v_mul_u32_u24_sdwa v148, v147, s61 dst_sel:DWORD dst_unused:UNUSED_PAD src0_sel:BYTE_0 src1_sel:DWORD\n"
      "v_mul_u32_u24_sdwa v149, v147, s61 dst_sel:DWORD dst_unused:UNUSED_PAD src0_sel:BYTE_1 src1_sel:DWORD\n"
      "v_mfma_f32_16x16x32_bf16 v[76:79], v[132:135], v[108:111], v[76:79]\n"
      "buffer_load_dwordx2 v[186:187], v21, s[12:15], s36 offen \n"
      "v_mul_u32_u24_sdwa v150, v147, s61 dst_sel:DWORD dst_unused:UNUSED_PAD src0_sel:BYTE_2 src1_sel:DWORD\n"
      "v_mul_u32_u24_sdwa v151, v147, s61 dst_sel:DWORD dst_unused:UNUSED_PAD src0_sel:BYTE_3 src1_sel:DWORD\n"
      "v_mfma_f32_16x16x32_bf16 v[48:51], v[136:139], v[24:27], v[48:51]\n"
      "v_mul_u32_u24_sdwa v144, v146, s61 dst_sel:DWORD dst_unused:UNUSED_PAD src0_sel:BYTE_0 src1_sel:DWORD\n"
      "v_mul_u32_u24_sdwa v145, v146, s61 dst_sel:DWORD dst_unused:UNUSED_PAD src0_sel:BYTE_1 src1_sel:DWORD\n"
      "v_mfma_f32_16x16x32_bf16 v[52:55], v[140:143], v[24:27], v[52:55]\n"
      "s_waitcnt vmcnt(10) lgkmcnt(0)\n"
      "s_barrier\n"
      "ds_read_b128 v[40:43], v4 offset:4096\n"
      "v_mfma_f32_16x16x32_bf16 v[56:59], v[136:139], v[28:31], v[56:59]\n"
      "s_add_u32 m0, s29, 7168\n"
      "v_mul_u32_u24_sdwa v147, v146, s61 dst_sel:DWORD dst_unused:UNUSED_PAD src0_sel:BYTE_3 src1_sel:DWORD\n"
      "v_mul_u32_u24_sdwa v146, v146, s61 dst_sel:DWORD dst_unused:UNUSED_PAD src0_sel:BYTE_2 src1_sel:DWORD\n"
      "v_mfma_f32_16x16x32_bf16 v[60:63], v[140:143], v[28:31], v[60:63]\n"
      "ds_read_b128 v[44:47], v4 offset:40960\n"
      "v_and_or_b32 v144, v144, s62, v10\n"
      "v_and_or_b32 v145, v145, s62, v10\n"
      "v_mfma_f32_16x16x32_bf16 v[64:67], v[136:139], v[32:35], v[64:67]\n"
      "ds_read_b128 v[88:91], v8 offset:2048\n"
      "v_and_or_b32 v147, v147, s62, v10\n"
      "v_and_or_b32 v146, v146, s62, v10\n"
      "v_mfma_f32_16x16x32_bf16 v[68:71], v[140:143], v[32:35], v[68:71]\n"
      "ds_read_b128 v[104:107], v13 offset:4096\n"
      "buffer_load_dwordx4 v1, s[8:11], s48 offen offset:0  lds\n"
      "v_and_or_b32 v148, v148, s62, v10\n"
      "v_and_or_b32 v149, v149, s62, v10\n"
      "v_mfma_f32_16x16x32_bf16 v[72:75], v[136:139], v[36:39], v[72:75]\n"
      "ds_read_b128 v[108:111], v13 offset:5120\n"
      "v_and_or_b32 v150, v150, s62, v10\n"
      "v_and_or_b32 v151, v151, s62, v10\n"
      "v_mfma_f32_16x16x32_bf16 v[76:79], v[140:143], v[36:39], v[76:79]\n"
      "s_waitcnt lgkmcnt(3)\n"
      "ds_read_b128 v[92:95], v9 offset:2048\n"
      "ds_read_b128 v[24:27], v13 offset:6144\n"
      "v_mfma_f32_16x16x32_bf16 v[48:51], v[144:147], v[40:43], v[48:51]\n"
      "ds_read_b128 v[28:31], v13 offset:7168\n"
      "ds_read_b128 v[32:35], v6 offset:4096\n"
      "buffer_load_dwordx4 v1, s[8:11], s49 offen offset:1024  lds\n"
      "v_mfma_f32_16x16x32_bf16 v[52:55], v[148:151], v[40:43], v[52:55]\n"
      "ds_read_b128 v[36:39], v6 offset:40960\n"
      "v_mul_u32_u24_sdwa v156, v155, s61 dst_sel:DWORD dst_unused:UNUSED_PAD src0_sel:BYTE_0 src1_sel:DWORD\n"
      "v_mul_u32_u24_sdwa v157, v155, s61 dst_sel:DWORD dst_unused:UNUSED_PAD src0_sel:BYTE_1 src1_sel:DWORD\n"
      "v_mfma_f32_16x16x32_bf16 v[56:59], v[144:147], v[44:47], v[56:59]\n"
      "ds_read_b128 v[40:43], v5 offset:4096\n"
      "v_mul_u32_u24_sdwa v158, v155, s61 dst_sel:DWORD dst_unused:UNUSED_PAD src0_sel:BYTE_2 src1_sel:DWORD\n"
      "v_mul_u32_u24_sdwa v159, v155, s61 dst_sel:DWORD dst_unused:UNUSED_PAD src0_sel:BYTE_3 src1_sel:DWORD\n"
      "v_mfma_f32_16x16x32_bf16 v[60:63], v[148:151], v[44:47], v[60:63]\n"
      "s_waitcnt lgkmcnt(4)\n"
      "v_mul_u32_u24_sdwa v152, v154, s61 dst_sel:DWORD dst_unused:UNUSED_PAD src0_sel:BYTE_0 src1_sel:DWORD\n"
      "v_mul_u32_u24_sdwa v153, v154, s61 dst_sel:DWORD dst_unused:UNUSED_PAD src0_sel:BYTE_1 src1_sel:DWORD\n"
      "v_mfma_f32_16x16x32_bf16 v[80:83], v[104:107], v[88:91], v[80:83]\n"
      "buffer_load_ushort v19, v23, s[12:15], s36 offen\n"
      "v_mul_u32_u24_sdwa v155, v154, s61 dst_sel:DWORD dst_unused:UNUSED_PAD src0_sel:BYTE_3 src1_sel:DWORD\n"
      "v_mul_u32_u24_sdwa v154, v154, s61 dst_sel:DWORD dst_unused:UNUSED_PAD src0_sel:BYTE_2 src1_sel:DWORD\n"
      "v_mfma_f32_16x16x32_bf16 v[84:87], v[108:111], v[88:91], v[84:87]\n"
      "ds_read_b128 v[44:47], v5 offset:40960\n"
      "v_and_or_b32 v152, v152, s62, v10\n"
      "v_and_or_b32 v153, v153, s62, v10\n"
      "v_mfma_f32_16x16x32_bf16 v[80:83], v[24:27], v[92:95], v[80:83]\n"
      "s_waitcnt lgkmcnt(1)\n"
      "v_and_or_b32 v155, v155, s62, v10\n"
      "v_and_or_b32 v154, v154, s62, v10\n"
      "v_mfma_f32_16x16x32_bf16 v[84:87], v[28:31], v[92:95], v[84:87]\n"
      "s_waitcnt vmcnt(12)\n"
      "v_mul_u32_u24_sdwa v16, v20, s61 dst_sel:DWORD dst_unused:UNUSED_PAD src0_sel:BYTE_0 src1_sel:DWORD\n"
      "v_mul_u32_u24_sdwa v17, v20, s61 dst_sel:DWORD dst_unused:UNUSED_PAD src0_sel:BYTE_1 src1_sel:DWORD\n"
      "v_smfmac_f32_16x16x64_bf16 v[96:99], v[100:103], v[88:95], v15\n"
      "ds_read_b128 v[104:107], v7 offset:4096\n"
      "v_and_or_b32 v16, v16, s62, v10\n"
      "v_and_or_b32 v17, v17, s62, v10\n"
      "v_mfma_f32_16x16x32_bf16 v[64:67], v[144:147], v[32:35], v[64:67]\n"
      "ds_write_b64 v14, v[16:17] offset:8192\n"
      "buffer_load_dwordx2 v[194:195], v2, s[12:15], s37 offen \n"
      "v_and_or_b32 v156, v156, s62, v10\n"
      "v_mfma_f32_16x16x32_bf16 v[68:71], v[148:151], v[32:35], v[68:71]\n"
      "ds_read_b128 v[108:111], v7 offset:40960\n"
      "v_and_or_b32 v157, v157, s62, v10\n"
      "v_and_or_b32 v158, v158, s62, v10\n"
      "v_mfma_f32_16x16x32_bf16 v[72:75], v[144:147], v[36:39], v[72:75]\n"
      "v_and_or_b32 v159, v159, s62, v10\n"
      "v_mfma_f32_16x16x32_bf16 v[76:79], v[148:151], v[36:39], v[76:79]\n"
      "s_waitcnt vmcnt(10) lgkmcnt(2)\n"
      "v_mul_u32_u24_sdwa v164, v163, s61 dst_sel:DWORD dst_unused:UNUSED_PAD src0_sel:BYTE_0 src1_sel:DWORD\n"
      "v_mul_u32_u24_sdwa v165, v163, s61 dst_sel:DWORD dst_unused:UNUSED_PAD src0_sel:BYTE_1 src1_sel:DWORD\n"
      "v_mfma_f32_16x16x32_bf16 v[48:51], v[152:155], v[40:43], v[48:51]\n"
      "buffer_load_dwordx2 v[202:203], v21, s[12:15], s37 offen \n"
      "v_mul_u32_u24_sdwa v166, v163, s61 dst_sel:DWORD dst_unused:UNUSED_PAD src0_sel:BYTE_2 src1_sel:DWORD\n"
      "v_mul_u32_u24_sdwa v167, v163, s61 dst_sel:DWORD dst_unused:UNUSED_PAD src0_sel:BYTE_3 src1_sel:DWORD\n"
      "v_mfma_f32_16x16x32_bf16 v[52:55], v[156:159], v[40:43], v[52:55]\n"
      "s_waitcnt vmcnt(8) lgkmcnt(0)\n"
      "s_barrier\n"
      "ds_read_b128 v[24:27], v4 offset:6144\n"
      "v_mul_u32_u24_sdwa v160, v162, s61 dst_sel:DWORD dst_unused:UNUSED_PAD src0_sel:BYTE_0 src1_sel:DWORD\n"
      "v_mul_u32_u24_sdwa v161, v162, s61 dst_sel:DWORD dst_unused:UNUSED_PAD src0_sel:BYTE_1 src1_sel:DWORD\n"
      "v_mfma_f32_16x16x32_bf16 v[56:59], v[152:155], v[44:47], v[56:59]\n"
      "ds_read_b128 v[28:31], v4 offset:43008\n"
      "v_mul_u32_u24_sdwa v163, v162, s61 dst_sel:DWORD dst_unused:UNUSED_PAD src0_sel:BYTE_3 src1_sel:DWORD\n"
      "v_mul_u32_u24_sdwa v162, v162, s61 dst_sel:DWORD dst_unused:UNUSED_PAD src0_sel:BYTE_2 src1_sel:DWORD\n"
      "v_mfma_f32_16x16x32_bf16 v[60:63], v[156:159], v[44:47], v[60:63]\n"
      "ds_read_b128 v[88:91], v8 offset:4096\n"
      "v_and_or_b32 v160, v160, s62, v10\n"
      "v_and_or_b32 v161, v161, s62, v10\n"
      "v_mfma_f32_16x16x32_bf16 v[64:67], v[152:155], v[104:107], v[64:67]\n"
      "ds_read_b128 v[32:35], v13 offset:8192\n"
      "v_and_or_b32 v163, v163, s62, v10\n"
      "v_and_or_b32 v162, v162, s62, v10\n"
      "v_mfma_f32_16x16x32_bf16 v[68:71], v[156:159], v[104:107], v[68:71]\n"
      "buffer_load_dwordx4 v1, s[8:11], s50 offen offset:2048  lds\n"
      "v_and_or_b32 v164, v164, s62, v10\n"
      "v_and_or_b32 v165, v165, s62, v10\n"
      "v_mfma_f32_16x16x32_bf16 v[72:75], v[152:155], v[108:111], v[72:75]\n"
      "ds_read_b128 v[36:39], v13 offset:9216\n"
      "v_and_or_b32 v166, v166, s62, v10\n"
      "v_and_or_b32 v167, v167, s62, v10\n"
      "v_mfma_f32_16x16x32_bf16 v[76:79], v[156:159], v[108:111], v[76:79]\n"
      "s_waitcnt lgkmcnt(2)\n"
      "ds_read_b128 v[92:95], v9 offset:4096\n"
      "ds_read_b128 v[40:43], v13 offset:10240\n"
      "v_mfma_f32_16x16x32_bf16 v[48:51], v[160:163], v[24:27], v[48:51]\n"
      "ds_read_b128 v[44:47], v13 offset:11264\n"
      "ds_read_b128 v[104:107], v6 offset:6144\n"
      "buffer_load_dwordx4 v1, s[8:11], s51 offen offset:3072  lds\n"
      "v_mfma_f32_16x16x32_bf16 v[52:55], v[164:167], v[24:27], v[52:55]\n"
      "ds_read_b128 v[108:111], v6 offset:43008\n"
      "v_mul_u32_u24_sdwa v172, v171, s61 dst_sel:DWORD dst_unused:UNUSED_PAD src0_sel:BYTE_0 src1_sel:DWORD\n"
      "v_mul_u32_u24_sdwa v173, v171, s61 dst_sel:DWORD dst_unused:UNUSED_PAD src0_sel:BYTE_1 src1_sel:DWORD\n"
      "v_mfma_f32_16x16x32_bf16 v[56:59], v[160:163], v[28:31], v[56:59]\n"
      "ds_read_b128 v[24:27], v5 offset:6144\n"
      "v_mul_u32_u24_sdwa v174, v171, s61 dst_sel:DWORD dst_unused:UNUSED_PAD src0_sel:BYTE_2 src1_sel:DWORD\n"
      "v_mul_u32_u24_sdwa v175, v171, s61 dst_sel:DWORD dst_unused:UNUSED_PAD src0_sel:BYTE_3 src1_sel:DWORD\n"
      "v_mfma_f32_16x16x32_bf16 v[60:63], v[164:167], v[28:31], v[60:63]\n"
      "s_waitcnt lgkmcnt(4)\n"
      "v_mul_u32_u24_sdwa v168, v170, s61 dst_sel:DWORD dst_unused:UNUSED_PAD src0_sel:BYTE_0 src1_sel:DWORD\n"
      "v_mul_u32_u24_sdwa v169, v170, s61 dst_sel:DWORD dst_unused:UNUSED_PAD src0_sel:BYTE_1 src1_sel:DWORD\n"
      "v_mfma_f32_16x16x32_bf16 v[80:83], v[32:35], v[88:91], v[80:83]\n"
      "buffer_load_ushort v20, v23, s[12:15], s37 offen\n"
      "v_mul_u32_u24_sdwa v171, v170, s61 dst_sel:DWORD dst_unused:UNUSED_PAD src0_sel:BYTE_3 src1_sel:DWORD\n"
      "v_mul_u32_u24_sdwa v170, v170, s61 dst_sel:DWORD dst_unused:UNUSED_PAD src0_sel:BYTE_2 src1_sel:DWORD\n"
      "v_mfma_f32_16x16x32_bf16 v[84:87], v[36:39], v[88:91], v[84:87]\n"
      "ds_read_b128 v[28:31], v5 offset:43008\n"
      "v_and_or_b32 v168, v168, s62, v10\n"
      "v_and_or_b32 v169, v169, s62, v10\n"
      "v_mfma_f32_16x16x32_bf16 v[80:83], v[40:43], v[92:95], v[80:83]\n"
      "s_waitcnt lgkmcnt(1)\n"
      "v_and_or_b32 v171, v171, s62, v10\n"
      "v_and_or_b32 v170, v170, s62, v10\n"
      "v_mfma_f32_16x16x32_bf16 v[84:87], v[44:47], v[92:95], v[84:87]\n"
      "s_waitcnt vmcnt(10)\n"
      "v_mul_u32_u24_sdwa v16, v18, s61 dst_sel:DWORD dst_unused:UNUSED_PAD src0_sel:BYTE_0 src1_sel:DWORD\n"
      "v_mul_u32_u24_sdwa v17, v18, s61 dst_sel:DWORD dst_unused:UNUSED_PAD src0_sel:BYTE_1 src1_sel:DWORD\n"
      "v_smfmac_f32_16x16x64_bf16 v[96:99], v[100:103], v[88:95], v15\n"
      "ds_read_b128 v[32:35], v7 offset:6144\n"
      "v_and_or_b32 v16, v16, s62, v10\n"
      "v_and_or_b32 v17, v17, s62, v10\n"
      "v_mfma_f32_16x16x32_bf16 v[64:67], v[160:163], v[104:107], v[64:67]\n"
      "ds_write_b64 v14, v[16:17] offset:0\n"
      "buffer_load_dwordx2 v[210:211], v2, s[12:15], s38 offen \n"
      "v_and_or_b32 v172, v172, s62, v10\n"
      "v_mfma_f32_16x16x32_bf16 v[68:71], v[164:167], v[104:107], v[68:71]\n"
      "ds_read_b128 v[36:39], v7 offset:43008\n"
      "v_and_or_b32 v173, v173, s62, v10\n"
      "v_and_or_b32 v174, v174, s62, v10\n"
      "v_mfma_f32_16x16x32_bf16 v[72:75], v[160:163], v[108:111], v[72:75]\n"
      "v_and_or_b32 v175, v175, s62, v10\n"
      "v_mfma_f32_16x16x32_bf16 v[76:79], v[164:167], v[108:111], v[76:79]\n"
      "s_waitcnt vmcnt(10) lgkmcnt(2)\n"
      "v_mul_u32_u24_sdwa v180, v179, s61 dst_sel:DWORD dst_unused:UNUSED_PAD src0_sel:BYTE_0 src1_sel:DWORD\n"
      "v_mul_u32_u24_sdwa v181, v179, s61 dst_sel:DWORD dst_unused:UNUSED_PAD src0_sel:BYTE_1 src1_sel:DWORD\n"
      "v_mfma_f32_16x16x32_bf16 v[48:51], v[168:171], v[24:27], v[48:51]\n"
      "buffer_load_dwordx2 v[218:219], v21, s[12:15], s38 offen \n"
      "v_mul_u32_u24_sdwa v182, v179, s61 dst_sel:DWORD dst_unused:UNUSED_PAD src0_sel:BYTE_2 src1_sel:DWORD\n"
      "v_mul_u32_u24_sdwa v183, v179, s61 dst_sel:DWORD dst_unused:UNUSED_PAD src0_sel:BYTE_3 src1_sel:DWORD\n"
      "v_mfma_f32_16x16x32_bf16 v[52:55], v[172:175], v[24:27], v[52:55]\n"
      "s_waitcnt vmcnt(8) lgkmcnt(0)\n"
      "s_barrier\n"
      "ds_read_b128 v[40:43], v4 offset:8192\n"
      "v_mul_u32_u24_sdwa v176, v178, s61 dst_sel:DWORD dst_unused:UNUSED_PAD src0_sel:BYTE_0 src1_sel:DWORD\n"
      "v_mul_u32_u24_sdwa v177, v178, s61 dst_sel:DWORD dst_unused:UNUSED_PAD src0_sel:BYTE_1 src1_sel:DWORD\n"
      "v_mfma_f32_16x16x32_bf16 v[56:59], v[168:171], v[28:31], v[56:59]\n"
      "s_add_u32 m0, s29, 11264\n"
      "v_mul_u32_u24_sdwa v179, v178, s61 dst_sel:DWORD dst_unused:UNUSED_PAD src0_sel:BYTE_3 src1_sel:DWORD\n"
      "v_mul_u32_u24_sdwa v178, v178, s61 dst_sel:DWORD dst_unused:UNUSED_PAD src0_sel:BYTE_2 src1_sel:DWORD\n"
      "v_mfma_f32_16x16x32_bf16 v[60:63], v[172:175], v[28:31], v[60:63]\n"
      "ds_read_b128 v[44:47], v4 offset:45056\n"
      "v_and_or_b32 v176, v176, s62, v10\n"
      "v_and_or_b32 v177, v177, s62, v10\n"
      "v_mfma_f32_16x16x32_bf16 v[64:67], v[168:171], v[32:35], v[64:67]\n"
      "ds_read_b128 v[88:91], v8 offset:6144\n"
      "v_and_or_b32 v179, v179, s62, v10\n"
      "v_and_or_b32 v178, v178, s62, v10\n"
      "v_mfma_f32_16x16x32_bf16 v[68:71], v[172:175], v[32:35], v[68:71]\n"
      "ds_read_b128 v[104:107], v13 offset:0\n"
      "buffer_load_dwordx4 v1, s[8:11], s52 offen offset:0  lds\n"
      "v_and_or_b32 v180, v180, s62, v10\n"
      "v_and_or_b32 v181, v181, s62, v10\n"
      "v_mfma_f32_16x16x32_bf16 v[72:75], v[168:171], v[36:39], v[72:75]\n"
      "ds_read_b128 v[108:111], v13 offset:1024\n"
      "v_and_or_b32 v182, v182, s62, v10\n"
      "v_and_or_b32 v183, v183, s62, v10\n"
      "v_mfma_f32_16x16x32_bf16 v[76:79], v[172:175], v[36:39], v[76:79]\n"
      "s_waitcnt lgkmcnt(3)\n"
      "ds_read_b128 v[92:95], v9 offset:6144\n"
      "ds_read_b128 v[24:27], v13 offset:2048\n"
      "v_mfma_f32_16x16x32_bf16 v[48:51], v[176:179], v[40:43], v[48:51]\n"
      "ds_read_b128 v[28:31], v13 offset:3072\n"
      "ds_read_b128 v[32:35], v6 offset:8192\n"
      "buffer_load_dwordx4 v1, s[8:11], s53 offen offset:1024  lds\n"
      "v_mfma_f32_16x16x32_bf16 v[52:55], v[180:183], v[40:43], v[52:55]\n"
      "ds_read_b128 v[36:39], v6 offset:45056\n"
      "v_mul_u32_u24_sdwa v188, v187, s61 dst_sel:DWORD dst_unused:UNUSED_PAD src0_sel:BYTE_0 src1_sel:DWORD\n"
      "v_mul_u32_u24_sdwa v189, v187, s61 dst_sel:DWORD dst_unused:UNUSED_PAD src0_sel:BYTE_1 src1_sel:DWORD\n"
      "v_mfma_f32_16x16x32_bf16 v[56:59], v[176:179], v[44:47], v[56:59]\n"
      "ds_read_b128 v[40:43], v5 offset:8192\n"
      "v_mul_u32_u24_sdwa v190, v187, s61 dst_sel:DWORD dst_unused:UNUSED_PAD src0_sel:BYTE_2 src1_sel:DWORD\n"
      "v_mul_u32_u24_sdwa v191, v187, s61 dst_sel:DWORD dst_unused:UNUSED_PAD src0_sel:BYTE_3 src1_sel:DWORD\n"
      "v_mfma_f32_16x16x32_bf16 v[60:63], v[180:183], v[44:47], v[60:63]\n"
      "s_waitcnt lgkmcnt(4)\n"
      "v_mul_u32_u24_sdwa v184, v186, s61 dst_sel:DWORD dst_unused:UNUSED_PAD src0_sel:BYTE_0 src1_sel:DWORD\n"
      "v_mul_u32_u24_sdwa v185, v186, s61 dst_sel:DWORD dst_unused:UNUSED_PAD src0_sel:BYTE_1 src1_sel:DWORD\n"
      "v_mfma_f32_16x16x32_bf16 v[80:83], v[104:107], v[88:91], v[80:83]\n"
      "buffer_load_ushort v18, v23, s[12:15], s38 offen\n"
      "v_mul_u32_u24_sdwa v187, v186, s61 dst_sel:DWORD dst_unused:UNUSED_PAD src0_sel:BYTE_3 src1_sel:DWORD\n"
      "v_mul_u32_u24_sdwa v186, v186, s61 dst_sel:DWORD dst_unused:UNUSED_PAD src0_sel:BYTE_2 src1_sel:DWORD\n"
      "v_mfma_f32_16x16x32_bf16 v[84:87], v[108:111], v[88:91], v[84:87]\n"
      "ds_read_b128 v[44:47], v5 offset:45056\n"
      "v_and_or_b32 v184, v184, s62, v10\n"
      "v_and_or_b32 v185, v185, s62, v10\n"
      "v_mfma_f32_16x16x32_bf16 v[80:83], v[24:27], v[92:95], v[80:83]\n"
      "s_waitcnt lgkmcnt(1)\n"
      "v_and_or_b32 v187, v187, s62, v10\n"
      "v_and_or_b32 v186, v186, s62, v10\n"
      "v_mfma_f32_16x16x32_bf16 v[84:87], v[28:31], v[92:95], v[84:87]\n"
      "s_waitcnt vmcnt(10)\n"
      "v_mul_u32_u24_sdwa v16, v19, s61 dst_sel:DWORD dst_unused:UNUSED_PAD src0_sel:BYTE_0 src1_sel:DWORD\n"
      "v_mul_u32_u24_sdwa v17, v19, s61 dst_sel:DWORD dst_unused:UNUSED_PAD src0_sel:BYTE_1 src1_sel:DWORD\n"
      "v_smfmac_f32_16x16x64_bf16 v[96:99], v[100:103], v[88:95], v15\n"
      "ds_read_b128 v[104:107], v7 offset:8192\n"
      "v_and_or_b32 v16, v16, s62, v10\n"
      "v_and_or_b32 v17, v17, s62, v10\n"
      "v_mfma_f32_16x16x32_bf16 v[64:67], v[176:179], v[32:35], v[64:67]\n"
      "ds_write_b64 v14, v[16:17] offset:4096\n"
      "buffer_load_dwordx2 v[226:227], v2, s[12:15], s39 offen \n"
      "v_and_or_b32 v188, v188, s62, v10\n"
      "v_mfma_f32_16x16x32_bf16 v[68:71], v[180:183], v[32:35], v[68:71]\n"
      "ds_read_b128 v[108:111], v7 offset:45056\n"
      "v_and_or_b32 v189, v189, s62, v10\n"
      "v_and_or_b32 v190, v190, s62, v10\n"
      "v_mfma_f32_16x16x32_bf16 v[72:75], v[176:179], v[36:39], v[72:75]\n"
      "v_and_or_b32 v191, v191, s62, v10\n"
      "v_mfma_f32_16x16x32_bf16 v[76:79], v[180:183], v[36:39], v[76:79]\n"
      "s_waitcnt vmcnt(10) lgkmcnt(2)\n"
      "v_mul_u32_u24_sdwa v196, v195, s61 dst_sel:DWORD dst_unused:UNUSED_PAD src0_sel:BYTE_0 src1_sel:DWORD\n"
      "v_mul_u32_u24_sdwa v197, v195, s61 dst_sel:DWORD dst_unused:UNUSED_PAD src0_sel:BYTE_1 src1_sel:DWORD\n"
      "v_mfma_f32_16x16x32_bf16 v[48:51], v[184:187], v[40:43], v[48:51]\n"
      "buffer_load_dwordx2 v[234:235], v21, s[12:15], s39 offen \n"
      "v_mul_u32_u24_sdwa v198, v195, s61 dst_sel:DWORD dst_unused:UNUSED_PAD src0_sel:BYTE_2 src1_sel:DWORD\n"
      "v_mul_u32_u24_sdwa v199, v195, s61 dst_sel:DWORD dst_unused:UNUSED_PAD src0_sel:BYTE_3 src1_sel:DWORD\n"
      "v_mfma_f32_16x16x32_bf16 v[52:55], v[188:191], v[40:43], v[52:55]\n"
      "s_waitcnt vmcnt(8) lgkmcnt(0)\n"
      "s_barrier\n"
      "ds_read_b128 v[24:27], v4 offset:10240\n"
      "v_mul_u32_u24_sdwa v192, v194, s61 dst_sel:DWORD dst_unused:UNUSED_PAD src0_sel:BYTE_0 src1_sel:DWORD\n"
      "v_mul_u32_u24_sdwa v193, v194, s61 dst_sel:DWORD dst_unused:UNUSED_PAD src0_sel:BYTE_1 src1_sel:DWORD\n"
      "v_mfma_f32_16x16x32_bf16 v[56:59], v[184:187], v[44:47], v[56:59]\n"
      "ds_read_b128 v[28:31], v4 offset:47104\n"
      "v_mul_u32_u24_sdwa v195, v194, s61 dst_sel:DWORD dst_unused:UNUSED_PAD src0_sel:BYTE_3 src1_sel:DWORD\n"
      "v_mul_u32_u24_sdwa v194, v194, s61 dst_sel:DWORD dst_unused:UNUSED_PAD src0_sel:BYTE_2 src1_sel:DWORD\n"
      "v_mfma_f32_16x16x32_bf16 v[60:63], v[188:191], v[44:47], v[60:63]\n"
      "ds_read_b128 v[88:91], v8 offset:8192\n"
      "v_and_or_b32 v192, v192, s62, v10\n"
      "v_and_or_b32 v193, v193, s62, v10\n"
      "v_mfma_f32_16x16x32_bf16 v[64:67], v[184:187], v[104:107], v[64:67]\n"
      "ds_read_b128 v[32:35], v13 offset:4096\n"
      "v_and_or_b32 v195, v195, s62, v10\n"
      "v_and_or_b32 v194, v194, s62, v10\n"
      "v_mfma_f32_16x16x32_bf16 v[68:71], v[188:191], v[104:107], v[68:71]\n"
      "buffer_load_dwordx4 v1, s[8:11], s54 offen offset:2048  lds\n"
      "v_and_or_b32 v196, v196, s62, v10\n"
      "v_and_or_b32 v197, v197, s62, v10\n"
      "v_mfma_f32_16x16x32_bf16 v[72:75], v[184:187], v[108:111], v[72:75]\n"
      "ds_read_b128 v[36:39], v13 offset:5120\n"
      "v_and_or_b32 v198, v198, s62, v10\n"
      "v_and_or_b32 v199, v199, s62, v10\n"
      "v_mfma_f32_16x16x32_bf16 v[76:79], v[188:191], v[108:111], v[76:79]\n"
      "s_waitcnt lgkmcnt(2)\n"
      "ds_read_b128 v[92:95], v9 offset:8192\n"
      "ds_read_b128 v[40:43], v13 offset:6144\n"
      "v_mfma_f32_16x16x32_bf16 v[48:51], v[192:195], v[24:27], v[48:51]\n"
      "ds_read_b128 v[44:47], v13 offset:7168\n"
      "ds_read_b128 v[104:107], v6 offset:10240\n"
      "buffer_load_dwordx4 v1, s[8:11], s55 offen offset:3072  lds\n"
      "v_mfma_f32_16x16x32_bf16 v[52:55], v[196:199], v[24:27], v[52:55]\n"
      "ds_read_b128 v[108:111], v6 offset:47104\n"
      "v_mul_u32_u24_sdwa v204, v203, s61 dst_sel:DWORD dst_unused:UNUSED_PAD src0_sel:BYTE_0 src1_sel:DWORD\n"
      "v_mul_u32_u24_sdwa v205, v203, s61 dst_sel:DWORD dst_unused:UNUSED_PAD src0_sel:BYTE_1 src1_sel:DWORD\n"
      "v_mfma_f32_16x16x32_bf16 v[56:59], v[192:195], v[28:31], v[56:59]\n"
      "ds_read_b128 v[24:27], v5 offset:10240\n"
      "v_mul_u32_u24_sdwa v206, v203, s61 dst_sel:DWORD dst_unused:UNUSED_PAD src0_sel:BYTE_2 src1_sel:DWORD\n"
      "v_mul_u32_u24_sdwa v207, v203, s61 dst_sel:DWORD dst_unused:UNUSED_PAD src0_sel:BYTE_3 src1_sel:DWORD\n"
      "v_mfma_f32_16x16x32_bf16 v[60:63], v[196:199], v[28:31], v[60:63]\n"
      "s_waitcnt lgkmcnt(4)\n"
      "v_mul_u32_u24_sdwa v200, v202, s61 dst_sel:DWORD dst_unused:UNUSED_PAD src0_sel:BYTE_0 src1_sel:DWORD\n"
      "v_mul_u32_u24_sdwa v201, v202, s61 dst_sel:DWORD dst_unused:UNUSED_PAD src0_sel:BYTE_1 src1_sel:DWORD\n"
      "v_mfma_f32_16x16x32_bf16 v[80:83], v[32:35], v[88:91], v[80:83]\n"
      "buffer_load_ushort v19, v23, s[12:15], s39 offen\n"
      "v_mul_u32_u24_sdwa v203, v202, s61 dst_sel:DWORD dst_unused:UNUSED_PAD src0_sel:BYTE_3 src1_sel:DWORD\n"
      "v_mul_u32_u24_sdwa v202, v202, s61 dst_sel:DWORD dst_unused:UNUSED_PAD src0_sel:BYTE_2 src1_sel:DWORD\n"
      "v_mfma_f32_16x16x32_bf16 v[84:87], v[36:39], v[88:91], v[84:87]\n"
      "ds_read_b128 v[28:31], v5 offset:47104\n"
      "v_and_or_b32 v200, v200, s62, v10\n"
      "v_and_or_b32 v201, v201, s62, v10\n"
      "v_mfma_f32_16x16x32_bf16 v[80:83], v[40:43], v[92:95], v[80:83]\n"
      "s_waitcnt lgkmcnt(1)\n"
      "v_and_or_b32 v203, v203, s62, v10\n"
      "v_and_or_b32 v202, v202, s62, v10\n"
      "v_mfma_f32_16x16x32_bf16 v[84:87], v[44:47], v[92:95], v[84:87]\n"
      "s_waitcnt vmcnt(10)\n"
      "v_mul_u32_u24_sdwa v16, v20, s61 dst_sel:DWORD dst_unused:UNUSED_PAD src0_sel:BYTE_0 src1_sel:DWORD\n"
      "v_mul_u32_u24_sdwa v17, v20, s61 dst_sel:DWORD dst_unused:UNUSED_PAD src0_sel:BYTE_1 src1_sel:DWORD\n"
      "v_smfmac_f32_16x16x64_bf16 v[96:99], v[100:103], v[88:95], v15\n"
      "ds_read_b128 v[32:35], v7 offset:10240\n"
      "v_and_or_b32 v16, v16, s62, v10\n"
      "v_and_or_b32 v17, v17, s62, v10\n"
      "v_mfma_f32_16x16x32_bf16 v[64:67], v[192:195], v[104:107], v[64:67]\n"
      "ds_write_b64 v14, v[16:17] offset:8192\n"
      "buffer_load_dwordx2 v[242:243], v2, s[12:15], s40 offen \n"
      "v_and_or_b32 v204, v204, s62, v10\n"
      "v_mfma_f32_16x16x32_bf16 v[68:71], v[196:199], v[104:107], v[68:71]\n"
      "ds_read_b128 v[36:39], v7 offset:47104\n"
      "v_and_or_b32 v205, v205, s62, v10\n"
      "v_and_or_b32 v206, v206, s62, v10\n"
      "v_mfma_f32_16x16x32_bf16 v[72:75], v[192:195], v[108:111], v[72:75]\n"
      "v_and_or_b32 v207, v207, s62, v10\n"
      "v_mfma_f32_16x16x32_bf16 v[76:79], v[196:199], v[108:111], v[76:79]\n"
      "s_waitcnt vmcnt(10) lgkmcnt(2)\n"
      "v_mul_u32_u24_sdwa v212, v211, s61 dst_sel:DWORD dst_unused:UNUSED_PAD src0_sel:BYTE_0 src1_sel:DWORD\n"
      "v_mul_u32_u24_sdwa v213, v211, s61 dst_sel:DWORD dst_unused:UNUSED_PAD src0_sel:BYTE_1 src1_sel:DWORD\n"
      "v_mfma_f32_16x16x32_bf16 v[48:51], v[200:203], v[24:27], v[48:51]\n"
      "buffer_load_dwordx2 v[250:251], v21, s[12:15], s40 offen \n"
      "v_mul_u32_u24_sdwa v214, v211, s61 dst_sel:DWORD dst_unused:UNUSED_PAD src0_sel:BYTE_2 src1_sel:DWORD\n"
      "v_mul_u32_u24_sdwa v215, v211, s61 dst_sel:DWORD dst_unused:UNUSED_PAD src0_sel:BYTE_3 src1_sel:DWORD\n"
      "v_mfma_f32_16x16x32_bf16 v[52:55], v[204:207], v[24:27], v[52:55]\n"
      "s_waitcnt vmcnt(8) lgkmcnt(0)\n"
      "s_barrier\n"
      "ds_read_b128 v[40:43], v4 offset:12288\n"
      "v_mul_u32_u24_sdwa v208, v210, s61 dst_sel:DWORD dst_unused:UNUSED_PAD src0_sel:BYTE_0 src1_sel:DWORD\n"
      "v_mul_u32_u24_sdwa v209, v210, s61 dst_sel:DWORD dst_unused:UNUSED_PAD src0_sel:BYTE_1 src1_sel:DWORD\n"
      "v_mfma_f32_16x16x32_bf16 v[56:59], v[200:203], v[28:31], v[56:59]\n"
      "s_add_u32 m0, s29, 15360\n"
      "v_mul_u32_u24_sdwa v211, v210, s61 dst_sel:DWORD dst_unused:UNUSED_PAD src0_sel:BYTE_3 src1_sel:DWORD\n"
      "v_mul_u32_u24_sdwa v210, v210, s61 dst_sel:DWORD dst_unused:UNUSED_PAD src0_sel:BYTE_2 src1_sel:DWORD\n"
      "v_mfma_f32_16x16x32_bf16 v[60:63], v[204:207], v[28:31], v[60:63]\n"
      "ds_read_b128 v[44:47], v4 offset:49152\n"
      "v_and_or_b32 v208, v208, s62, v10\n"
      "v_and_or_b32 v209, v209, s62, v10\n"
      "v_mfma_f32_16x16x32_bf16 v[64:67], v[200:203], v[32:35], v[64:67]\n"
      "ds_read_b128 v[88:91], v8 offset:10240\n"
      "v_and_or_b32 v211, v211, s62, v10\n"
      "v_and_or_b32 v210, v210, s62, v10\n"
      "v_mfma_f32_16x16x32_bf16 v[68:71], v[204:207], v[32:35], v[68:71]\n"
      "ds_read_b128 v[104:107], v13 offset:8192\n"
      "buffer_load_dwordx4 v1, s[8:11], s56 offen offset:0  lds\n"
      "v_and_or_b32 v212, v212, s62, v10\n"
      "v_and_or_b32 v213, v213, s62, v10\n"
      "v_mfma_f32_16x16x32_bf16 v[72:75], v[200:203], v[36:39], v[72:75]\n"
      "ds_read_b128 v[108:111], v13 offset:9216\n"
      "v_and_or_b32 v214, v214, s62, v10\n"
      "v_and_or_b32 v215, v215, s62, v10\n"
      "v_mfma_f32_16x16x32_bf16 v[76:79], v[204:207], v[36:39], v[76:79]\n"
      "s_waitcnt lgkmcnt(3)\n"
      "ds_read_b128 v[92:95], v9 offset:10240\n"
      "ds_read_b128 v[24:27], v13 offset:10240\n"
      "v_mfma_f32_16x16x32_bf16 v[48:51], v[208:211], v[40:43], v[48:51]\n"
      "ds_read_b128 v[28:31], v13 offset:11264\n"
      "ds_read_b128 v[32:35], v6 offset:12288\n"
      "buffer_load_dwordx4 v1, s[8:11], s57 offen offset:1024  lds\n"
      "v_mfma_f32_16x16x32_bf16 v[52:55], v[212:215], v[40:43], v[52:55]\n"
      "ds_read_b128 v[36:39], v6 offset:49152\n"
      "v_mul_u32_u24_sdwa v220, v219, s61 dst_sel:DWORD dst_unused:UNUSED_PAD src0_sel:BYTE_0 src1_sel:DWORD\n"
      "v_mul_u32_u24_sdwa v221, v219, s61 dst_sel:DWORD dst_unused:UNUSED_PAD src0_sel:BYTE_1 src1_sel:DWORD\n"
      "v_mfma_f32_16x16x32_bf16 v[56:59], v[208:211], v[44:47], v[56:59]\n"
      "ds_read_b128 v[40:43], v5 offset:12288\n"
      "v_mul_u32_u24_sdwa v222, v219, s61 dst_sel:DWORD dst_unused:UNUSED_PAD src0_sel:BYTE_2 src1_sel:DWORD\n"
      "v_mul_u32_u24_sdwa v223, v219, s61 dst_sel:DWORD dst_unused:UNUSED_PAD src0_sel:BYTE_3 src1_sel:DWORD\n"
      "v_mfma_f32_16x16x32_bf16 v[60:63], v[212:215], v[44:47], v[60:63]\n"
      "s_waitcnt lgkmcnt(4)\n"
      "v_mul_u32_u24_sdwa v216, v218, s61 dst_sel:DWORD dst_unused:UNUSED_PAD src0_sel:BYTE_0 src1_sel:DWORD\n"
      "v_mul_u32_u24_sdwa v217, v218, s61 dst_sel:DWORD dst_unused:UNUSED_PAD src0_sel:BYTE_1 src1_sel:DWORD\n"
      "v_mfma_f32_16x16x32_bf16 v[80:83], v[104:107], v[88:91], v[80:83]\n"
      "buffer_load_ushort v20, v23, s[12:15], s40 offen\n"
      "v_mul_u32_u24_sdwa v219, v218, s61 dst_sel:DWORD dst_unused:UNUSED_PAD src0_sel:BYTE_3 src1_sel:DWORD\n"
      "v_mul_u32_u24_sdwa v218, v218, s61 dst_sel:DWORD dst_unused:UNUSED_PAD src0_sel:BYTE_2 src1_sel:DWORD\n"
      "v_mfma_f32_16x16x32_bf16 v[84:87], v[108:111], v[88:91], v[84:87]\n"
      "ds_read_b128 v[44:47], v5 offset:49152\n"
      "v_and_or_b32 v216, v216, s62, v10\n"
      "v_and_or_b32 v217, v217, s62, v10\n"
      "v_mfma_f32_16x16x32_bf16 v[80:83], v[24:27], v[92:95], v[80:83]\n"
      "s_waitcnt lgkmcnt(1)\n"
      "v_and_or_b32 v219, v219, s62, v10\n"
      "v_and_or_b32 v218, v218, s62, v10\n"
      "v_mfma_f32_16x16x32_bf16 v[84:87], v[28:31], v[92:95], v[84:87]\n"
      "s_waitcnt vmcnt(10)\n"
      "v_mul_u32_u24_sdwa v16, v18, s61 dst_sel:DWORD dst_unused:UNUSED_PAD src0_sel:BYTE_0 src1_sel:DWORD\n"
      "v_mul_u32_u24_sdwa v17, v18, s61 dst_sel:DWORD dst_unused:UNUSED_PAD src0_sel:BYTE_1 src1_sel:DWORD\n"
      "v_smfmac_f32_16x16x64_bf16 v[96:99], v[100:103], v[88:95], v15\n"
      "ds_read_b128 v[104:107], v7 offset:12288\n"
      "v_and_or_b32 v16, v16, s62, v10\n"
      "v_and_or_b32 v17, v17, s62, v10\n"
      "v_mfma_f32_16x16x32_bf16 v[64:67], v[208:211], v[32:35], v[64:67]\n"
      "ds_write_b64 v14, v[16:17] offset:0\n"
      "buffer_load_dwordx4 v1, s[8:11], s58 offen offset:2048  lds\n"
      "v_and_or_b32 v220, v220, s62, v10\n"
      "v_mfma_f32_16x16x32_bf16 v[68:71], v[212:215], v[32:35], v[68:71]\n"
      "ds_read_b128 v[108:111], v7 offset:49152\n"
      "v_and_or_b32 v221, v221, s62, v10\n"
      "v_and_or_b32 v222, v222, s62, v10\n"
      "v_mfma_f32_16x16x32_bf16 v[72:75], v[208:211], v[36:39], v[72:75]\n"
      "v_and_or_b32 v223, v223, s62, v10\n"
      "v_mfma_f32_16x16x32_bf16 v[76:79], v[212:215], v[36:39], v[76:79]\n"
      "s_waitcnt vmcnt(10) lgkmcnt(2)\n"
      "v_mul_u32_u24_sdwa v228, v227, s61 dst_sel:DWORD dst_unused:UNUSED_PAD src0_sel:BYTE_0 src1_sel:DWORD\n"
      "v_mul_u32_u24_sdwa v229, v227, s61 dst_sel:DWORD dst_unused:UNUSED_PAD src0_sel:BYTE_1 src1_sel:DWORD\n"
      "v_mfma_f32_16x16x32_bf16 v[48:51], v[216:219], v[40:43], v[48:51]\n"
      "v_mul_u32_u24_sdwa v230, v227, s61 dst_sel:DWORD dst_unused:UNUSED_PAD src0_sel:BYTE_2 src1_sel:DWORD\n"
      "v_mul_u32_u24_sdwa v231, v227, s61 dst_sel:DWORD dst_unused:UNUSED_PAD src0_sel:BYTE_3 src1_sel:DWORD\n"
      "v_mfma_f32_16x16x32_bf16 v[52:55], v[220:223], v[40:43], v[52:55]\n"
      "s_waitcnt vmcnt(7) lgkmcnt(0)\n"
      "s_barrier\n"
      "ds_read_b128 v[24:27], v4 offset:14336\n"
      "v_mul_u32_u24_sdwa v224, v226, s61 dst_sel:DWORD dst_unused:UNUSED_PAD src0_sel:BYTE_0 src1_sel:DWORD\n"
      "v_mul_u32_u24_sdwa v225, v226, s61 dst_sel:DWORD dst_unused:UNUSED_PAD src0_sel:BYTE_1 src1_sel:DWORD\n"
      "v_mfma_f32_16x16x32_bf16 v[56:59], v[216:219], v[44:47], v[56:59]\n"
      "ds_read_b128 v[28:31], v4 offset:51200\n"
      "v_mul_u32_u24_sdwa v227, v226, s61 dst_sel:DWORD dst_unused:UNUSED_PAD src0_sel:BYTE_3 src1_sel:DWORD\n"
      "v_mul_u32_u24_sdwa v226, v226, s61 dst_sel:DWORD dst_unused:UNUSED_PAD src0_sel:BYTE_2 src1_sel:DWORD\n"
      "v_mfma_f32_16x16x32_bf16 v[60:63], v[220:223], v[44:47], v[60:63]\n"
      "ds_read_b128 v[88:91], v8 offset:12288\n"
      "v_and_or_b32 v224, v224, s62, v10\n"
      "v_and_or_b32 v225, v225, s62, v10\n"
      "v_mfma_f32_16x16x32_bf16 v[64:67], v[216:219], v[104:107], v[64:67]\n"
      "ds_read_b128 v[32:35], v13 offset:0\n"
      "v_and_or_b32 v227, v227, s62, v10\n"
      "v_and_or_b32 v226, v226, s62, v10\n"
      "v_mfma_f32_16x16x32_bf16 v[68:71], v[220:223], v[104:107], v[68:71]\n"
      "ds_read_b128 v[36:39], v13 offset:1024\n"
      "v_and_or_b32 v228, v228, s62, v10\n"
      "v_and_or_b32 v229, v229, s62, v10\n"
      "v_mfma_f32_16x16x32_bf16 v[72:75], v[216:219], v[108:111], v[72:75]\n"
      "ds_read_b128 v[92:95], v9 offset:12288\n"
      "v_and_or_b32 v230, v230, s62, v10\n"
      "v_and_or_b32 v231, v231, s62, v10\n"
      "v_mfma_f32_16x16x32_bf16 v[76:79], v[220:223], v[108:111], v[76:79]\n"
      "s_waitcnt lgkmcnt(3)\n"
      "ds_read_b128 v[40:43], v13 offset:2048\n"
      "ds_read_b128 v[44:47], v13 offset:3072\n"
      "v_mfma_f32_16x16x32_bf16 v[48:51], v[224:227], v[24:27], v[48:51]\n"
      "ds_read_b128 v[104:107], v6 offset:14336\n"
      "v_mul_u32_u24_sdwa v236, v235, s61 dst_sel:DWORD dst_unused:UNUSED_PAD src0_sel:BYTE_0 src1_sel:DWORD\n"
      "v_mul_u32_u24_sdwa v237, v235, s61 dst_sel:DWORD dst_unused:UNUSED_PAD src0_sel:BYTE_1 src1_sel:DWORD\n"
      "v_mfma_f32_16x16x32_bf16 v[52:55], v[228:231], v[24:27], v[52:55]\n"
      "ds_read_b128 v[108:111], v6 offset:51200\n"
      "v_mul_u32_u24_sdwa v238, v235, s61 dst_sel:DWORD dst_unused:UNUSED_PAD src0_sel:BYTE_2 src1_sel:DWORD\n"
      "v_mul_u32_u24_sdwa v239, v235, s61 dst_sel:DWORD dst_unused:UNUSED_PAD src0_sel:BYTE_3 src1_sel:DWORD\n"
      "v_mfma_f32_16x16x32_bf16 v[56:59], v[224:227], v[28:31], v[56:59]\n"
      "ds_read_b128 v[24:27], v5 offset:14336\n"
      "v_mul_u32_u24_sdwa v232, v234, s61 dst_sel:DWORD dst_unused:UNUSED_PAD src0_sel:BYTE_0 src1_sel:DWORD\n"
      "v_mul_u32_u24_sdwa v233, v234, s61 dst_sel:DWORD dst_unused:UNUSED_PAD src0_sel:BYTE_1 src1_sel:DWORD\n"
      "v_mfma_f32_16x16x32_bf16 v[60:63], v[228:231], v[28:31], v[60:63]\n"
      "s_waitcnt lgkmcnt(3)\n"
      "v_mul_u32_u24_sdwa v235, v234, s61 dst_sel:DWORD dst_unused:UNUSED_PAD src0_sel:BYTE_3 src1_sel:DWORD\n"
      "v_mul_u32_u24_sdwa v234, v234, s61 dst_sel:DWORD dst_unused:UNUSED_PAD src0_sel:BYTE_2 src1_sel:DWORD\n"
      "v_mfma_f32_16x16x32_bf16 v[80:83], v[32:35], v[88:91], v[80:83]\n"
      "ds_read_b128 v[28:31], v5 offset:51200\n"
      "v_and_or_b32 v232, v232, s62, v10\n"
      "v_and_or_b32 v233, v233, s62, v10\n"
      "v_mfma_f32_16x16x32_bf16 v[84:87], v[36:39], v[88:91], v[84:87]\n"
      "ds_read_b128 v[32:35], v7 offset:14336\n"
      "v_and_or_b32 v235, v235, s62, v10\n"
      "v_and_or_b32 v234, v234, s62, v10\n"
      "v_mfma_f32_16x16x32_bf16 v[80:83], v[40:43], v[92:95], v[80:83]\n"
      "s_waitcnt vmcnt(6) lgkmcnt(2)\n"
      "v_mul_u32_u24_sdwa v16, v19, s61 dst_sel:DWORD dst_unused:UNUSED_PAD src0_sel:BYTE_0 src1_sel:DWORD\n"
      "v_mul_u32_u24_sdwa v17, v19, s61 dst_sel:DWORD dst_unused:UNUSED_PAD src0_sel:BYTE_1 src1_sel:DWORD\n"
      "v_mfma_f32_16x16x32_bf16 v[84:87], v[44:47], v[92:95], v[84:87]\n"
      "ds_read_b128 v[36:39], v7 offset:51200\n"
      "v_and_or_b32 v16, v16, s62, v10\n"
      "v_and_or_b32 v17, v17, s62, v10\n"
      "v_smfmac_f32_16x16x64_bf16 v[96:99], v[100:103], v[88:95], v15\n"
      "ds_write_b64 v14, v[16:17] offset:4096\n"
      "s_waitcnt vmcnt(1) lgkmcnt(3)\n"
      "v_mul_u32_u24_sdwa v16, v20, s61 dst_sel:DWORD dst_unused:UNUSED_PAD src0_sel:BYTE_0 src1_sel:DWORD\n"
      "v_mul_u32_u24_sdwa v17, v20, s61 dst_sel:DWORD dst_unused:UNUSED_PAD src0_sel:BYTE_1 src1_sel:DWORD\n"
      "v_mfma_f32_16x16x32_bf16 v[64:67], v[224:227], v[104:107], v[64:67]\n"
      "v_and_or_b32 v16, v16, s62, v10\n"
      "v_and_or_b32 v17, v17, s62, v10\n"
      "v_mfma_f32_16x16x32_bf16 v[68:71], v[228:231], v[104:107], v[68:71]\n"
      "ds_write_b64 v14, v[16:17] offset:8192\n"
      "v_and_or_b32 v236, v236, s62, v10\n"
      "v_and_or_b32 v237, v237, s62, v10\n"
      "v_mfma_f32_16x16x32_bf16 v[72:75], v[224:227], v[108:111], v[72:75]\n"
      "v_and_or_b32 v238, v238, s62, v10\n"
      "v_and_or_b32 v239, v239, s62, v10\n"
      "v_mfma_f32_16x16x32_bf16 v[76:79], v[228:231], v[108:111], v[76:79]\n"
      "v_mul_u32_u24_sdwa v244, v243, s61 dst_sel:DWORD dst_unused:UNUSED_PAD src0_sel:BYTE_0 src1_sel:DWORD\n"
      "v_mul_u32_u24_sdwa v245, v243, s61 dst_sel:DWORD dst_unused:UNUSED_PAD src0_sel:BYTE_1 src1_sel:DWORD\n"
      "v_mfma_f32_16x16x32_bf16 v[48:51], v[232:235], v[24:27], v[48:51]\n"
      "v_mul_u32_u24_sdwa v246, v243, s61 dst_sel:DWORD dst_unused:UNUSED_PAD src0_sel:BYTE_2 src1_sel:DWORD\n"
      "v_mul_u32_u24_sdwa v247, v243, s61 dst_sel:DWORD dst_unused:UNUSED_PAD src0_sel:BYTE_3 src1_sel:DWORD\n"
      "v_mfma_f32_16x16x32_bf16 v[52:55], v[236:239], v[24:27], v[52:55]\n"
      "s_waitcnt vmcnt(0) lgkmcnt(0)\n"
      "s_barrier\n"
      "ds_read_b128 v[40:43], v4 offset:16384\n"
      "v_mul_u32_u24_sdwa v240, v242, s61 dst_sel:DWORD dst_unused:UNUSED_PAD src0_sel:BYTE_0 src1_sel:DWORD\n"
      "v_mul_u32_u24_sdwa v241, v242, s61 dst_sel:DWORD dst_unused:UNUSED_PAD src0_sel:BYTE_1 src1_sel:DWORD\n"
      "v_mfma_f32_16x16x32_bf16 v[56:59], v[232:235], v[28:31], v[56:59]\n"
      "ds_read_b128 v[88:91], v8 offset:14336\n"
      "v_mul_u32_u24_sdwa v243, v242, s61 dst_sel:DWORD dst_unused:UNUSED_PAD src0_sel:BYTE_3 src1_sel:DWORD\n"
      "v_mul_u32_u24_sdwa v242, v242, s61 dst_sel:DWORD dst_unused:UNUSED_PAD src0_sel:BYTE_2 src1_sel:DWORD\n"
      "v_mfma_f32_16x16x32_bf16 v[60:63], v[236:239], v[28:31], v[60:63]\n"
      "ds_read_b128 v[44:47], v13 offset:4096\n"
      "v_and_or_b32 v240, v240, s62, v10\n"
      "v_and_or_b32 v241, v241, s62, v10\n"
      "v_mfma_f32_16x16x32_bf16 v[64:67], v[232:235], v[32:35], v[64:67]\n"
      "ds_read_b128 v[104:107], v13 offset:5120\n"
      "v_and_or_b32 v243, v243, s62, v10\n"
      "v_and_or_b32 v242, v242, s62, v10\n"
      "v_mfma_f32_16x16x32_bf16 v[68:71], v[236:239], v[32:35], v[68:71]\n"
      "ds_read_b128 v[92:95], v9 offset:14336\n"
      "ds_read_b128 v[108:111], v13 offset:6144\n"
      "v_and_or_b32 v244, v244, s62, v10\n"
      "v_and_or_b32 v245, v245, s62, v10\n"
      "v_mfma_f32_16x16x32_bf16 v[72:75], v[232:235], v[36:39], v[72:75]\n"
      "ds_read_b128 v[24:27], v13 offset:7168\n"
      "v_and_or_b32 v246, v246, s62, v10\n"
      "v_and_or_b32 v247, v247, s62, v10\n"
      "v_mfma_f32_16x16x32_bf16 v[76:79], v[236:239], v[36:39], v[76:79]\n"
      "s_waitcnt lgkmcnt(4)\n"
      "ds_read_b128 v[28:31], v4 offset:53248\n"
      "ds_read_b128 v[32:35], v6 offset:16384\n"
      "v_mfma_f32_16x16x32_bf16 v[48:51], v[240:243], v[40:43], v[48:51]\n"
      "ds_read_b128 v[36:39], v13 offset:8192\n"
      "v_mul_u32_u24_sdwa v252, v251, s61 dst_sel:DWORD dst_unused:UNUSED_PAD src0_sel:BYTE_0 src1_sel:DWORD\n"
      "v_mul_u32_u24_sdwa v253, v251, s61 dst_sel:DWORD dst_unused:UNUSED_PAD src0_sel:BYTE_1 src1_sel:DWORD\n"
      "v_mfma_f32_16x16x32_bf16 v[52:55], v[244:247], v[40:43], v[52:55]\n"
      "ds_read_b128 v[40:43], v13 offset:9216\n"
      "v_mul_u32_u24_sdwa v254, v251, s61 dst_sel:DWORD dst_unused:UNUSED_PAD src0_sel:BYTE_2 src1_sel:DWORD\n"
      "v_mul_u32_u24_sdwa v255, v251, s61 dst_sel:DWORD dst_unused:UNUSED_PAD src0_sel:BYTE_3 src1_sel:DWORD\n"
      "v_mfma_f32_16x16x32_bf16 v[80:83], v[44:47], v[88:91], v[80:83]\n"
      "s_waitcnt lgkmcnt(4)\n"
      "ds_read_b128 v[44:47], v13 offset:10240\n"
      "v_mul_u32_u24_sdwa v248, v250, s61 dst_sel:DWORD dst_unused:UNUSED_PAD src0_sel:BYTE_0 src1_sel:DWORD\n"
      "v_mfma_f32_16x16x32_bf16 v[84:87], v[104:107], v[88:91], v[84:87]\n"
      "ds_read_b128 v[104:107], v13 offset:11264\n"
      "v_mul_u32_u24_sdwa v249, v250, s61 dst_sel:DWORD dst_unused:UNUSED_PAD src0_sel:BYTE_1 src1_sel:DWORD\n"
      "v_mul_u32_u24_sdwa v251, v250, s61 dst_sel:DWORD dst_unused:UNUSED_PAD src0_sel:BYTE_3 src1_sel:DWORD\n"
      "v_mfma_f32_16x16x32_bf16 v[80:83], v[108:111], v[92:95], v[80:83]\n"
      "ds_read_b128 v[108:111], v6 offset:53248\n"
      "v_mul_u32_u24_sdwa v250, v250, s61 dst_sel:DWORD dst_unused:UNUSED_PAD src0_sel:BYTE_2 src1_sel:DWORD\n"
      "v_and_or_b32 v248, v248, s62, v10\n"
      "v_mfma_f32_16x16x32_bf16 v[84:87], v[24:27], v[92:95], v[84:87]\n"
      "ds_read_b128 v[24:27], v5 offset:16384\n"
      "v_and_or_b32 v249, v249, s62, v10\n"
      "v_and_or_b32 v251, v251, s62, v10\n"
      "v_smfmac_f32_16x16x64_bf16 v[96:99], v[100:103], v[88:95], v15\n"
      "s_waitcnt lgkmcnt(3)\n"
      "ds_read_b128 v[88:91], v8 offset:16384\n"
      "ds_read_b128 v[92:95], v9 offset:16384\n"
      "v_mfma_f32_16x16x32_bf16 v[56:59], v[240:243], v[28:31], v[56:59]\n"
      "v_and_or_b32 v250, v250, s62, v10\n"
      "v_and_or_b32 v252, v252, s62, v10\n"
      "v_mfma_f32_16x16x32_bf16 v[60:63], v[244:247], v[28:31], v[60:63]\n"
      "ds_read_b128 v[28:31], v5 offset:53248\n"
      "v_and_or_b32 v253, v253, s62, v10\n"
      "v_and_or_b32 v254, v254, s62, v10\n"
      "v_mfma_f32_16x16x32_bf16 v[64:67], v[240:243], v[32:35], v[64:67]\n"
      "v_and_or_b32 v255, v255, s62, v10\n"
      "v_mfma_f32_16x16x32_bf16 v[68:71], v[244:247], v[32:35], v[68:71]\n"
      "s_waitcnt lgkmcnt(1)\n"
      "ds_read_b128 v[32:35], v7 offset:16384\n"
      "v_mfma_f32_16x16x32_bf16 v[80:83], v[36:39], v[88:91], v[80:83]\n"
      "ds_read_b128 v[36:39], v7 offset:53248\n"
      "v_mfma_f32_16x16x32_bf16 v[84:87], v[40:43], v[88:91], v[84:87]\n"
      "v_mfma_f32_16x16x32_bf16 v[80:83], v[44:47], v[92:95], v[80:83]\n"
      "ds_read_b128 v[40:43], v4 offset:1024\n"
      "v_mfma_f32_16x16x32_bf16 v[84:87], v[104:107], v[92:95], v[84:87]\n"
      "v_smfmac_f32_16x16x64_bf16 v[96:99], v[100:103], v[88:95], v15\n"
      "ds_read_b128 v[44:47], v5 offset:1024\n"
      "v_mfma_f32_16x16x32_bf16 v[72:75], v[240:243], v[108:111], v[72:75]\n"
      "v_mfma_f32_16x16x32_bf16 v[76:79], v[244:247], v[108:111], v[76:79]\n"
      "ds_read_b128 v[104:107], v4 offset:3072\n"
      "v_mfma_f32_16x16x32_bf16 v[48:51], v[248:251], v[24:27], v[48:51]\n"
      "s_nop 2\n"
      "v_add_f32 v16, v96, v97\n"
      "v_add_u32 v17, s68, v22\n"
      "v_mfma_f32_16x16x32_bf16 v[52:55], v[252:255], v[24:27], v[52:55]\n"
      "s_waitcnt lgkmcnt(1)\n"
      "ds_write_b32 v17, v16\n"
      "ds_read_b128 v[108:111], v5 offset:3072\n"
      "v_mfma_f32_16x16x32_bf16 v[56:59], v[248:251], v[28:31], v[56:59]\n"
      "v_lshlrev_b32 v11, 16, v11\n"
      "v_lshlrev_b32 v12, 16, v12\n"
      "v_mfma_f32_16x16x32_bf16 v[60:63], v[252:255], v[28:31], v[60:63]\n"
      "s_waitcnt lgkmcnt(1)\n"
      "s_barrier\n"
      "ds_read_b128 v[96:99], v22\n"
      "v_mfma_f32_16x16x32_bf16 v[64:67], v[248:251], v[32:35], v[64:67]\n"
      "ds_read_b128 v[24:27], v4 offset:5120\n"
      "ds_read_b128 v[100:103], v22 offset:16\n"
      "v_sub_f32 v12, 0xc1800000, v12\n"
      "v_mfma_f32_16x16x32_bf16 v[68:71], v[252:255], v[32:35], v[68:71]\n"
      "ds_read_b128 v[28:31], v5 offset:5120\n"
      "v_fmac_f32_dpp v80, v12, v16 row_newbcast:8 row_mask:0xf bank_mask:0xf\n"
      "v_fmac_f32_dpp v81, v12, v16 row_newbcast:10 row_mask:0xf bank_mask:0xf\n"
      "v_mfma_f32_16x16x32_bf16 v[72:75], v[248:251], v[36:39], v[72:75]\n"
      "v_fmac_f32_dpp v82, v12, v16 row_newbcast:12 row_mask:0xf bank_mask:0xf\n"
      "v_fmac_f32_dpp v83, v12, v16 row_newbcast:14 row_mask:0xf bank_mask:0xf\n"
      "v_mfma_f32_16x16x32_bf16 v[76:79], v[252:255], v[36:39], v[76:79]\n"
      "s_setprio 0\n"
      "ds_read_b128 v[32:35], v4 offset:7168\n"
      "v_fmac_f32_dpp v84, v12, v16 row_newbcast:9 row_mask:0xf bank_mask:0xf\n"
      "v_fmac_f32_dpp v85, v12, v16 row_newbcast:11 row_mask:0xf bank_mask:0xf\n"
      "v_mfma_f32_16x16x32_bf16 v[88:91], v[112:115], v[40:43], 0\n"
      "ds_read_b128 v[36:39], v5 offset:7168\n"
      "v_fmac_f32_dpp v86, v12, v16 row_newbcast:13 row_mask:0xf bank_mask:0xf\n"
      "v_mfma_f32_16x16x32_bf16 v[92:95], v[116:119], v[40:43], 0\n"
      "ds_read_b128 v[40:43], v4 offset:9216\n"
      "v_fmac_f32_dpp v87, v12, v16 row_newbcast:15 row_mask:0xf bank_mask:0xf\n"
      "v_mfma_f32_16x16x32_bf16 v[88:91], v[120:123], v[44:47], v[88:91]\n"
      "v_mul_f32_dpp v80, v11, v80 row_newbcast:8 row_mask:0xf bank_mask:0xf\n"
      "v_mul_f32_dpp v84, v11, v84 row_newbcast:9 row_mask:0xf bank_mask:0xf\n"
      "v_mfma_f32_16x16x32_bf16 v[92:95], v[124:127], v[44:47], v[92:95]\n"
      "ds_read_b128 v[44:47], v5 offset:9216\n"
      "v_mul_f32_dpp v81, v11, v81 row_newbcast:10 row_mask:0xf bank_mask:0xf\n"
      "v_mfma_f32_16x16x32_bf16 v[88:91], v[128:131], v[104:107], v[88:91]\n"
      "v_mul_f32_dpp v85, v11, v85 row_newbcast:11 row_mask:0xf bank_mask:0xf\n"
      "v_mul_f32_dpp v82, v11, v82 row_newbcast:12 row_mask:0xf bank_mask:0xf\n"
      "v_mfma_f32_16x16x32_bf16 v[92:95], v[132:135], v[104:107], v[92:95]\n"
      "s_waitcnt lgkmcnt(2)\n"
      "ds_read_b128 v[104:107], v4 offset:11264\n"
      "v_mfma_f32_16x16x32_bf16 v[88:91], v[136:139], v[108:111], v[88:91]\n"
      "v_mul_f32_dpp v86, v11, v86 row_newbcast:13 row_mask:0xf bank_mask:0xf\n"
      "v_mul_f32_dpp v83, v11, v83 row_newbcast:14 row_mask:0xf bank_mask:0xf\n"
      "v_mfma_f32_16x16x32_bf16 v[92:95], v[140:143], v[108:111], v[92:95]\n"
      "ds_read_b128 v[108:111], v5 offset:11264\n"
      "v_mul_f32_dpp v87, v11, v87 row_newbcast:15 row_mask:0xf bank_mask:0xf\n"
      "v_mfma_f32_16x16x32_bf16 v[88:91], v[144:147], v[24:27], v[88:91]\n"
      "v_cvt_pk_bf16_f32 v80, v80, v84\n"
      "v_cvt_pk_bf16_f32 v81, v81, v85\n"
      "v_mfma_f32_16x16x32_bf16 v[92:95], v[148:151], v[24:27], v[92:95]\n"
      "ds_read_b128 v[24:27], v4 offset:13312\n"
      "v_cvt_pk_bf16_f32 v82, v82, v86\n"
      "v_mfma_f32_16x16x32_bf16 v[88:91], v[152:155], v[28:31], v[88:91]\n"
      "v_cvt_pk_bf16_f32 v83, v83, v87\n"
      "v_mfma_f32_16x16x32_bf16 v[92:95], v[156:159], v[28:31], v[92:95]\n"
      "ds_read_b128 v[28:31], v5 offset:13312\n"
      "buffer_store_dwordx4 v[80:83], v3, s[16:19], s64 offen sc0 sc1 nt\n"
      "v_mfma_f32_16x16x32_bf16 v[88:91], v[160:163], v[32:35], v[88:91]\n"
      "v_fmac_f32_dpp v48, v12, v96 row_newbcast:0 row_mask:0xf bank_mask:0xf\n"
      "v_fmac_f32_dpp v49, v12, v96 row_newbcast:2 row_mask:0xf bank_mask:0xf\n"
      "v_mfma_f32_16x16x32_bf16 v[92:95], v[164:167], v[32:35], v[92:95]\n"
      "v_fmac_f32_dpp v50, v12, v96 row_newbcast:4 row_mask:0xf bank_mask:0xf\n"
      "v_fmac_f32_dpp v51, v12, v96 row_newbcast:6 row_mask:0xf bank_mask:0xf\n"
      "v_mfma_f32_16x16x32_bf16 v[88:91], v[168:171], v[36:39], v[88:91]\n"
      "v_fmac_f32_dpp v52, v12, v96 row_newbcast:1 row_mask:0xf bank_mask:0xf\n"
      "v_fmac_f32_dpp v53, v12, v96 row_newbcast:3 row_mask:0xf bank_mask:0xf\n"
      "v_mfma_f32_16x16x32_bf16 v[92:95], v[172:175], v[36:39], v[92:95]\n"
      "s_waitcnt lgkmcnt(1)\n"
      "ds_read_b128 v[32:35], v4 offset:15360\n"
      "v_mfma_f32_16x16x32_bf16 v[88:91], v[176:179], v[40:43], v[88:91]\n"
      "v_fmac_f32_dpp v54, v12, v96 row_newbcast:5 row_mask:0xf bank_mask:0xf\n"
      "v_fmac_f32_dpp v55, v12, v96 row_newbcast:7 row_mask:0xf bank_mask:0xf\n"
      "v_mfma_f32_16x16x32_bf16 v[92:95], v[180:183], v[40:43], v[92:95]\n"
      "ds_read_b128 v[36:39], v5 offset:15360\n"
      "v_mul_f32_dpp v48, v11, v48 row_newbcast:0 row_mask:0xf bank_mask:0xf\n"
      "v_mfma_f32_16x16x32_bf16 v[88:91], v[184:187], v[44:47], v[88:91]\n"
      "v_mul_f32_dpp v52, v11, v52 row_newbcast:1 row_mask:0xf bank_mask:0xf\n"
      "v_mul_f32_dpp v49, v11, v49 row_newbcast:2 row_mask:0xf bank_mask:0xf\n"
      "v_mfma_f32_16x16x32_bf16 v[92:95], v[188:191], v[44:47], v[92:95]\n"
      "ds_read_b128 v[40:43], v4 offset:17408\n"
      "v_mul_f32_dpp v53, v11, v53 row_newbcast:3 row_mask:0xf bank_mask:0xf\n"
      "v_mfma_f32_16x16x32_bf16 v[88:91], v[192:195], v[104:107], v[88:91]\n"
      "v_mul_f32_dpp v50, v11, v50 row_newbcast:4 row_mask:0xf bank_mask:0xf\n"
      "v_mul_f32_dpp v54, v11, v54 row_newbcast:5 row_mask:0xf bank_mask:0xf\n"
      "v_mfma_f32_16x16x32_bf16 v[92:95], v[196:199], v[104:107], v[92:95]\n"
      "ds_read_b128 v[44:47], v5 offset:17408\n"
      "v_mul_f32_dpp v51, v11, v51 row_newbcast:6 row_mask:0xf bank_mask:0xf\n"
      "v_mfma_f32_16x16x32_bf16 v[88:91], v[200:203], v[108:111], v[88:91]\n"
      "v_mul_f32_dpp v55, v11, v55 row_newbcast:7 row_mask:0xf bank_mask:0xf\n"
      "v_cvt_pk_bf16_f32 v48, v48, v52\n"
      "v_mfma_f32_16x16x32_bf16 v[92:95], v[204:207], v[108:111], v[92:95]\n"
      "ds_read_b128 v[104:107], v4 offset:37888\n"
      "v_cvt_pk_bf16_f32 v49, v49, v53\n"
      "v_mfma_f32_16x16x32_bf16 v[88:91], v[208:211], v[24:27], v[88:91]\n"
      "v_cvt_pk_bf16_f32 v50, v50, v54\n"
      "v_cvt_pk_bf16_f32 v51, v51, v55\n"
      "v_mfma_f32_16x16x32_bf16 v[92:95], v[212:215], v[24:27], v[92:95]\n"
      "s_waitcnt lgkmcnt(2)\n"
      "buffer_store_dwordx4 v[48:51], v3, s[16:19], s63 offen sc0 sc1 nt\n"
      "v_mfma_f32_16x16x32_bf16 v[88:91], v[216:219], v[28:31], v[88:91]\n"
      "ds_read_b128 v[108:111], v5 offset:37888\n"
      "s_add_u32 s63, s63, 147456\n"
      "v_mfma_f32_16x16x32_bf16 v[92:95], v[220:223], v[28:31], v[92:95]\n"
      "ds_read_b128 v[24:27], v4 offset:39936\n"
      "v_fmac_f32_dpp v56, v12, v97 row_newbcast:0 row_mask:0xf bank_mask:0xf\n"
      "v_mfma_f32_16x16x32_bf16 v[88:91], v[224:227], v[32:35], v[88:91]\n"
      "v_fmac_f32_dpp v57, v12, v97 row_newbcast:2 row_mask:0xf bank_mask:0xf\n"
      "v_fmac_f32_dpp v58, v12, v97 row_newbcast:4 row_mask:0xf bank_mask:0xf\n"
      "v_mfma_f32_16x16x32_bf16 v[92:95], v[228:231], v[32:35], v[92:95]\n"
      "ds_read_b128 v[28:31], v5 offset:39936\n"
      "v_fmac_f32_dpp v59, v12, v97 row_newbcast:6 row_mask:0xf bank_mask:0xf\n"
      "v_mfma_f32_16x16x32_bf16 v[88:91], v[232:235], v[36:39], v[88:91]\n"
      "v_fmac_f32_dpp v60, v12, v97 row_newbcast:1 row_mask:0xf bank_mask:0xf\n"
      "v_fmac_f32_dpp v61, v12, v97 row_newbcast:3 row_mask:0xf bank_mask:0xf\n"
      "v_mfma_f32_16x16x32_bf16 v[92:95], v[236:239], v[36:39], v[92:95]\n"
      "ds_read_b128 v[32:35], v4 offset:41984\n"
      "v_fmac_f32_dpp v62, v12, v97 row_newbcast:5 row_mask:0xf bank_mask:0xf\n"
      "v_mfma_f32_16x16x32_bf16 v[88:91], v[240:243], v[40:43], v[88:91]\n"
      "v_fmac_f32_dpp v63, v12, v97 row_newbcast:7 row_mask:0xf bank_mask:0xf\n"
      "v_mul_f32_dpp v56, v11, v56 row_newbcast:0 row_mask:0xf bank_mask:0xf\n"
      "v_mfma_f32_16x16x32_bf16 v[92:95], v[244:247], v[40:43], v[92:95]\n"
      "s_waitcnt lgkmcnt(2)\n"
      "ds_read_b128 v[36:39], v5 offset:41984\n"
      "v_mfma_f32_16x16x32_bf16 v[88:91], v[248:251], v[44:47], v[88:91]\n"
      "v_mul_f32_dpp v60, v11, v60 row_newbcast:1 row_mask:0xf bank_mask:0xf\n"
      "v_mul_f32_dpp v57, v11, v57 row_newbcast:2 row_mask:0xf bank_mask:0xf\n"
      "v_mfma_f32_16x16x32_bf16 v[92:95], v[252:255], v[44:47], v[92:95]\n"
      "ds_read_b128 v[40:43], v4 offset:44032\n"
      "v_mul_f32_dpp v61, v11, v61 row_newbcast:3 row_mask:0xf bank_mask:0xf\n"
      "v_mfma_f32_16x16x32_bf16 v[80:83], v[112:115], v[104:107], 0\n"
      "v_mul_f32_dpp v58, v11, v58 row_newbcast:4 row_mask:0xf bank_mask:0xf\n"
      "v_mul_f32_dpp v62, v11, v62 row_newbcast:5 row_mask:0xf bank_mask:0xf\n"
      "v_mfma_f32_16x16x32_bf16 v[84:87], v[116:119], v[104:107], 0\n"
      "ds_read_b128 v[44:47], v5 offset:44032\n"
      "v_mul_f32_dpp v59, v11, v59 row_newbcast:6 row_mask:0xf bank_mask:0xf\n"
      "v_mfma_f32_16x16x32_bf16 v[80:83], v[120:123], v[108:111], v[80:83]\n"
      "v_mul_f32_dpp v63, v11, v63 row_newbcast:7 row_mask:0xf bank_mask:0xf\n"
      "v_cvt_pk_bf16_f32 v56, v56, v60\n"
      "v_mfma_f32_16x16x32_bf16 v[84:87], v[124:127], v[108:111], v[84:87]\n"
      "ds_read_b128 v[104:107], v4 offset:46080\n"
      "v_cvt_pk_bf16_f32 v57, v57, v61\n"
      "v_mfma_f32_16x16x32_bf16 v[80:83], v[128:131], v[24:27], v[80:83]\n"
      "v_cvt_pk_bf16_f32 v58, v58, v62\n"
      "v_cvt_pk_bf16_f32 v59, v59, v63\n"
      "v_mfma_f32_16x16x32_bf16 v[84:87], v[132:135], v[24:27], v[84:87]\n"
      "s_waitcnt lgkmcnt(2)\n"
      "buffer_store_dwordx4 v[56:59], v3, s[16:19], s63 offen sc0 sc1 nt\n"
      "v_mfma_f32_16x16x32_bf16 v[80:83], v[136:139], v[28:31], v[80:83]\n"
      "ds_read_b128 v[108:111], v5 offset:46080\n"
      "s_add_u32 s63, s63, 147456\n"
      "v_mfma_f32_16x16x32_bf16 v[84:87], v[140:143], v[28:31], v[84:87]\n"
      "ds_read_b128 v[24:27], v4 offset:48128\n"
      "v_fmac_f32_dpp v64, v12, v98 row_newbcast:0 row_mask:0xf bank_mask:0xf\n"
      "v_mfma_f32_16x16x32_bf16 v[80:83], v[144:147], v[32:35], v[80:83]\n"
      "v_fmac_f32_dpp v65, v12, v98 row_newbcast:2 row_mask:0xf bank_mask:0xf\n"
      "v_fmac_f32_dpp v66, v12, v98 row_newbcast:4 row_mask:0xf bank_mask:0xf\n"
      "v_mfma_f32_16x16x32_bf16 v[84:87], v[148:151], v[32:35], v[84:87]\n"
      "ds_read_b128 v[28:31], v5 offset:48128\n"
      "v_fmac_f32_dpp v67, v12, v98 row_newbcast:6 row_mask:0xf bank_mask:0xf\n"
      "v_mfma_f32_16x16x32_bf16 v[80:83], v[152:155], v[36:39], v[80:83]\n"
      "v_fmac_f32_dpp v68, v12, v98 row_newbcast:1 row_mask:0xf bank_mask:0xf\n"
      "v_fmac_f32_dpp v69, v12, v98 row_newbcast:3 row_mask:0xf bank_mask:0xf\n"
      "v_mfma_f32_16x16x32_bf16 v[84:87], v[156:159], v[36:39], v[84:87]\n"
      "ds_read_b128 v[32:35], v4 offset:50176\n"
      "v_fmac_f32_dpp v70, v12, v98 row_newbcast:5 row_mask:0xf bank_mask:0xf\n"
      "v_mfma_f32_16x16x32_bf16 v[80:83], v[160:163], v[40:43], v[80:83]\n"
      "v_fmac_f32_dpp v71, v12, v98 row_newbcast:7 row_mask:0xf bank_mask:0xf\n"
      "v_mul_f32_dpp v64, v11, v64 row_newbcast:0 row_mask:0xf bank_mask:0xf\n"
      "v_mfma_f32_16x16x32_bf16 v[84:87], v[164:167], v[40:43], v[84:87]\n"
      "s_waitcnt lgkmcnt(2)\n"
      "ds_read_b128 v[36:39], v5 offset:50176\n"
      "v_mfma_f32_16x16x32_bf16 v[80:83], v[168:171], v[44:47], v[80:83]\n"
      "v_mul_f32_dpp v68, v11, v68 row_newbcast:1 row_mask:0xf bank_mask:0xf\n"
      "v_mul_f32_dpp v65, v11, v65 row_newbcast:2 row_mask:0xf bank_mask:0xf\n"
      "v_mfma_f32_16x16x32_bf16 v[84:87], v[172:175], v[44:47], v[84:87]\n"
      "ds_read_b128 v[40:43], v4 offset:52224\n"
      "v_mul_f32_dpp v69, v11, v69 row_newbcast:3 row_mask:0xf bank_mask:0xf\n"
      "v_mfma_f32_16x16x32_bf16 v[80:83], v[176:179], v[104:107], v[80:83]\n"
      "v_mul_f32_dpp v66, v11, v66 row_newbcast:4 row_mask:0xf bank_mask:0xf\n"
      "v_mul_f32_dpp v70, v11, v70 row_newbcast:5 row_mask:0xf bank_mask:0xf\n"
      "v_mfma_f32_16x16x32_bf16 v[84:87], v[180:183], v[104:107], v[84:87]\n"
      "ds_read_b128 v[44:47], v5 offset:52224\n"
      "v_mul_f32_dpp v67, v11, v67 row_newbcast:6 row_mask:0xf bank_mask:0xf\n"
      "v_mfma_f32_16x16x32_bf16 v[80:83], v[184:187], v[108:111], v[80:83]\n"
      "v_mul_f32_dpp v71, v11, v71 row_newbcast:7 row_mask:0xf bank_mask:0xf\n"
      "v_cvt_pk_bf16_f32 v64, v64, v68\n"
      "v_mfma_f32_16x16x32_bf16 v[84:87], v[188:191], v[108:111], v[84:87]\n"
      "ds_read_b128 v[104:107], v4 offset:54272\n"
      "v_cvt_pk_bf16_f32 v65, v65, v69\n"
      "v_mfma_f32_16x16x32_bf16 v[80:83], v[192:195], v[24:27], v[80:83]\n"
      "v_cvt_pk_bf16_f32 v66, v66, v70\n"
      "v_cvt_pk_bf16_f32 v67, v67, v71\n"
      "v_mfma_f32_16x16x32_bf16 v[84:87], v[196:199], v[24:27], v[84:87]\n"
      "s_waitcnt lgkmcnt(2)\n"
      "buffer_store_dwordx4 v[64:67], v3, s[16:19], s63 offen sc0 sc1 nt\n"
      "v_mfma_f32_16x16x32_bf16 v[80:83], v[200:203], v[28:31], v[80:83]\n"
      "ds_read_b128 v[108:111], v5 offset:54272\n"
      "s_add_u32 s63, s63, 147456\n"
      "v_mfma_f32_16x16x32_bf16 v[84:87], v[204:207], v[28:31], v[84:87]\n"
      "ds_read_b128 v[24:27], v6 offset:1024\n"
      "v_mul_f32_dpp v56, v12, v103 row_newbcast:0 row_mask:0xf bank_mask:0xf\n"
      "v_mfma_f32_16x16x32_bf16 v[80:83], v[208:211], v[32:35], v[80:83]\n"
      "v_mul_f32_dpp v57, v12, v103 row_newbcast:2 row_mask:0xf bank_mask:0xf\n"
      "v_mul_f32_dpp v58, v12, v103 row_newbcast:4 row_mask:0xf bank_mask:0xf\n"
      "v_mfma_f32_16x16x32_bf16 v[84:87], v[212:215], v[32:35], v[84:87]\n"
      "ds_read_b128 v[28:31], v7 offset:1024\n"
      "v_mul_f32_dpp v59, v12, v103 row_newbcast:6 row_mask:0xf bank_mask:0xf\n"
      "v_mfma_f32_16x16x32_bf16 v[80:83], v[216:219], v[36:39], v[80:83]\n"
      "v_mul_f32_dpp v60, v12, v103 row_newbcast:1 row_mask:0xf bank_mask:0xf\n"
      "v_mul_f32_dpp v61, v12, v103 row_newbcast:3 row_mask:0xf bank_mask:0xf\n"
      "v_mfma_f32_16x16x32_bf16 v[84:87], v[220:223], v[36:39], v[84:87]\n"
      "ds_read_b128 v[32:35], v6 offset:3072\n"
      "v_mul_f32_dpp v62, v12, v103 row_newbcast:5 row_mask:0xf bank_mask:0xf\n"
      "v_mfma_f32_16x16x32_bf16 v[80:83], v[224:227], v[40:43], v[80:83]\n"
      "ds_read_b128 v[36:39], v7 offset:3072\n"
      "v_mul_f32_dpp v63, v12, v103 row_newbcast:7 row_mask:0xf bank_mask:0xf\n"
      "v_mfma_f32_16x16x32_bf16 v[84:87], v[228:231], v[40:43], v[84:87]\n"
      "s_waitcnt lgkmcnt(3)\n"
      "v_fmac_f32_dpp v72, v12, v99 row_newbcast:0 row_mask:0xf bank_mask:0xf\n"
      "v_mfma_f32_16x16x32_bf16 v[80:83], v[232:235], v[44:47], v[80:83]\n"
      "v_fmac_f32_dpp v73, v12, v99 row_newbcast:2 row_mask:0xf bank_mask:0xf\n"
      "v_fmac_f32_dpp v74, v12, v99 row_newbcast:4 row_mask:0xf bank_mask:0xf\n"
      "v_mfma_f32_16x16x32_bf16 v[84:87], v[236:239], v[44:47], v[84:87]\n"
      "ds_read_b128 v[40:43], v6 offset:5120\n"
      "v_fmac_f32_dpp v75, v12, v99 row_newbcast:6 row_mask:0xf bank_mask:0xf\n"
      "v_mfma_f32_16x16x32_bf16 v[80:83], v[240:243], v[104:107], v[80:83]\n"
      "v_fmac_f32_dpp v76, v12, v99 row_newbcast:1 row_mask:0xf bank_mask:0xf\n"
      "v_fmac_f32_dpp v77, v12, v99 row_newbcast:3 row_mask:0xf bank_mask:0xf\n"
      "v_mfma_f32_16x16x32_bf16 v[84:87], v[244:247], v[104:107], v[84:87]\n"
      "ds_read_b128 v[44:47], v7 offset:5120\n"
      "v_fmac_f32_dpp v78, v12, v99 row_newbcast:5 row_mask:0xf bank_mask:0xf\n"
      "v_mfma_f32_16x16x32_bf16 v[80:83], v[248:251], v[108:111], v[80:83]\n"
      "v_fmac_f32_dpp v79, v12, v99 row_newbcast:7 row_mask:0xf bank_mask:0xf\n"
      "v_mul_f32_dpp v72, v11, v72 row_newbcast:0 row_mask:0xf bank_mask:0xf\n"
      "v_mfma_f32_16x16x32_bf16 v[84:87], v[252:255], v[108:111], v[84:87]\n"
      "ds_read_b128 v[104:107], v6 offset:7168\n"
      "v_mul_f32_dpp v76, v11, v76 row_newbcast:1 row_mask:0xf bank_mask:0xf\n"
      "v_mfma_f32_16x16x32_bf16 v[48:51], v[112:115], v[24:27], 0\n"
      "v_mul_f32_dpp v73, v11, v73 row_newbcast:2 row_mask:0xf bank_mask:0xf\n"
      "v_mul_f32_dpp v77, v11, v77 row_newbcast:3 row_mask:0xf bank_mask:0xf\n"
      "v_mfma_f32_16x16x32_bf16 v[52:55], v[116:119], v[24:27], 0\n"
      "s_waitcnt lgkmcnt(2)\n"
      "ds_read_b128 v[108:111], v7 offset:7168\n"
      "v_mfma_f32_16x16x32_bf16 v[48:51], v[120:123], v[28:31], v[48:51]\n"
      "v_mul_f32_dpp v74, v11, v74 row_newbcast:4 row_mask:0xf bank_mask:0xf\n"
      "v_mul_f32_dpp v78, v11, v78 row_newbcast:5 row_mask:0xf bank_mask:0xf\n"
      "v_mfma_f32_16x16x32_bf16 v[52:55], v[124:127], v[28:31], v[52:55]\n"
      "ds_read_b128 v[24:27], v6 offset:9216\n"
      "v_mul_f32_dpp v75, v11, v75 row_newbcast:6 row_mask:0xf bank_mask:0xf\n"
      "v_mfma_f32_16x16x32_bf16 v[48:51], v[128:131], v[32:35], v[48:51]\n"
      "v_mul_f32_dpp v79, v11, v79 row_newbcast:7 row_mask:0xf bank_mask:0xf\n"
      "v_cvt_pk_bf16_f32 v72, v72, v76\n"
      "v_mfma_f32_16x16x32_bf16 v[52:55], v[132:135], v[32:35], v[52:55]\n"
      "ds_read_b128 v[28:31], v7 offset:9216\n"
      "v_cvt_pk_bf16_f32 v73, v73, v77\n"
      "v_mfma_f32_16x16x32_bf16 v[48:51], v[136:139], v[36:39], v[48:51]\n"
      "v_cvt_pk_bf16_f32 v74, v74, v78\n"
      "v_cvt_pk_bf16_f32 v75, v75, v79\n"
      "v_mfma_f32_16x16x32_bf16 v[52:55], v[140:143], v[36:39], v[52:55]\n"
      "buffer_store_dwordx4 v[72:75], v3, s[16:19], s63 offen sc0 sc1 nt\n"
      "s_add_u32 s63, s63, 147456\n"
      "v_mfma_f32_16x16x32_bf16 v[48:51], v[144:147], v[40:43], v[48:51]\n"
      "ds_read_b128 v[32:35], v6 offset:11264\n"
      "v_fmac_f32_dpp v88, v12, v100 row_newbcast:0 row_mask:0xf bank_mask:0xf\n"
      "v_mfma_f32_16x16x32_bf16 v[52:55], v[148:151], v[40:43], v[52:55]\n"
      "s_waitcnt lgkmcnt(2)\n"
      "ds_read_b128 v[36:39], v7 offset:11264\n"
      "v_mfma_f32_16x16x32_bf16 v[48:51], v[152:155], v[44:47], v[48:51]\n"
      "v_fmac_f32_dpp v89, v12, v100 row_newbcast:2 row_mask:0xf bank_mask:0xf\n"
      "v_fmac_f32_dpp v90, v12, v100 row_newbcast:4 row_mask:0xf bank_mask:0xf\n"
      "v_mfma_f32_16x16x32_bf16 v[52:55], v[156:159], v[44:47], v[52:55]\n"
      "ds_read_b128 v[40:43], v6 offset:13312\n"
      "v_fmac_f32_dpp v91, v12, v100 row_newbcast:6 row_mask:0xf bank_mask:0xf\n"
      "v_mfma_f32_16x16x32_bf16 v[48:51], v[160:163], v[104:107], v[48:51]\n"
      "v_fmac_f32_dpp v92, v12, v100 row_newbcast:1 row_mask:0xf bank_mask:0xf\n"
      "v_fmac_f32_dpp v93, v12, v100 row_newbcast:3 row_mask:0xf bank_mask:0xf\n"
      "v_mfma_f32_16x16x32_bf16 v[52:55], v[164:167], v[104:107], v[52:55]\n"
      "ds_read_b128 v[44:47], v7 offset:13312\n"
      "v_fmac_f32_dpp v94, v12, v100 row_newbcast:5 row_mask:0xf bank_mask:0xf\n"
      "v_mfma_f32_16x16x32_bf16 v[48:51], v[168:171], v[108:111], v[48:51]\n"
      "v_fmac_f32_dpp v95, v12, v100 row_newbcast:7 row_mask:0xf bank_mask:0xf\n"
      "v_mul_f32_dpp v88, v11, v88 row_newbcast:0 row_mask:0xf bank_mask:0xf\n"
      "v_mfma_f32_16x16x32_bf16 v[52:55], v[172:175], v[108:111], v[52:55]\n"
      "ds_read_b128 v[104:107], v6 offset:15360\n"
      "v_mul_f32_dpp v92, v11, v92 row_newbcast:1 row_mask:0xf bank_mask:0xf\n"
      "v_mfma_f32_16x16x32_bf16 v[48:51], v[176:179], v[24:27], v[48:51]\n"
      "v_mul_f32_dpp v89, v11, v89 row_newbcast:2 row_mask:0xf bank_mask:0xf\n"
      "v_mul_f32_dpp v93, v11, v93 row_newbcast:3 row_mask:0xf bank_mask:0xf\n"
      "v_mfma_f32_16x16x32_bf16 v[52:55], v[180:183], v[24:27], v[52:55]\n"
      "s_waitcnt lgkmcnt(2)\n"
      "ds_read_b128 v[108:111], v7 offset:15360\n"
      "v_mfma_f32_16x16x32_bf16 v[48:51], v[184:187], v[28:31], v[48:51]\n"
      "v_mul_f32_dpp v90, v11, v90 row_newbcast:4 row_mask:0xf bank_mask:0xf\n"
      "v_mul_f32_dpp v94, v11, v94 row_newbcast:5 row_mask:0xf bank_mask:0xf\n"
      "v_mfma_f32_16x16x32_bf16 v[52:55], v[188:191], v[28:31], v[52:55]\n"
      "ds_read_b128 v[24:27], v6 offset:17408\n"
      "v_mul_f32_dpp v91, v11, v91 row_newbcast:6 row_mask:0xf bank_mask:0xf\n"
      "v_mfma_f32_16x16x32_bf16 v[48:51], v[192:195], v[32:35], v[48:51]\n"
      "v_mul_f32_dpp v95, v11, v95 row_newbcast:7 row_mask:0xf bank_mask:0xf\n"
      "v_cvt_pk_bf16_f32 v88, v88, v92\n"
      "v_mfma_f32_16x16x32_bf16 v[52:55], v[196:199], v[32:35], v[52:55]\n"
      "ds_read_b128 v[28:31], v7 offset:17408\n"
      "v_cvt_pk_bf16_f32 v89, v89, v93\n"
      "v_mfma_f32_16x16x32_bf16 v[48:51], v[200:203], v[36:39], v[48:51]\n"
      "v_cvt_pk_bf16_f32 v90, v90, v94\n"
      "v_cvt_pk_bf16_f32 v91, v91, v95\n"
      "v_mfma_f32_16x16x32_bf16 v[52:55], v[204:207], v[36:39], v[52:55]\n"
      "buffer_store_dwordx4 v[88:91], v3, s[16:19], s63 offen sc0 sc1 nt\n"
      "s_add_u32 s63, s63, 147456\n"
      "v_mfma_f32_16x16x32_bf16 v[48:51], v[208:211], v[40:43], v[48:51]\n"
      "ds_read_b128 v[32:35], v6 offset:37888\n"
      "v_fmac_f32_dpp v80, v12, v101 row_newbcast:0 row_mask:0xf bank_mask:0xf\n"
      "v_mfma_f32_16x16x32_bf16 v[52:55], v[212:215], v[40:43], v[52:55]\n"
      "s_waitcnt lgkmcnt(2)\n"
      "ds_read_b128 v[36:39], v7 offset:37888\n"
      "v_mfma_f32_16x16x32_bf16 v[48:51], v[216:219], v[44:47], v[48:51]\n"
      "v_fmac_f32_dpp v81, v12, v101 row_newbcast:2 row_mask:0xf bank_mask:0xf\n"
      "v_fmac_f32_dpp v82, v12, v101 row_newbcast:4 row_mask:0xf bank_mask:0xf\n"
      "v_mfma_f32_16x16x32_bf16 v[52:55], v[220:223], v[44:47], v[52:55]\n"
      "ds_read_b128 v[40:43], v6 offset:39936\n"
      "v_fmac_f32_dpp v83, v12, v101 row_newbcast:6 row_mask:0xf bank_mask:0xf\n"
      "v_mfma_f32_16x16x32_bf16 v[48:51], v[224:227], v[104:107], v[48:51]\n"
      "v_fmac_f32_dpp v84, v12, v101 row_newbcast:1 row_mask:0xf bank_mask:0xf\n"
      "v_fmac_f32_dpp v85, v12, v101 row_newbcast:3 row_mask:0xf bank_mask:0xf\n"
      "v_mfma_f32_16x16x32_bf16 v[52:55], v[228:231], v[104:107], v[52:55]\n"
      "ds_read_b128 v[44:47], v7 offset:39936\n"
      "v_fmac_f32_dpp v86, v12, v101 row_newbcast:5 row_mask:0xf bank_mask:0xf\n"
      "v_mfma_f32_16x16x32_bf16 v[48:51], v[232:235], v[108:111], v[48:51]\n"
      "v_fmac_f32_dpp v87, v12, v101 row_newbcast:7 row_mask:0xf bank_mask:0xf\n"
      "v_mul_f32_dpp v80, v11, v80 row_newbcast:0 row_mask:0xf bank_mask:0xf\n"
      "v_mfma_f32_16x16x32_bf16 v[52:55], v[236:239], v[108:111], v[52:55]\n"
      "ds_read_b128 v[104:107], v6 offset:41984\n"
      "v_mul_f32_dpp v84, v11, v84 row_newbcast:1 row_mask:0xf bank_mask:0xf\n"
      "v_mfma_f32_16x16x32_bf16 v[48:51], v[240:243], v[24:27], v[48:51]\n"
      "v_mul_f32_dpp v81, v11, v81 row_newbcast:2 row_mask:0xf bank_mask:0xf\n"
      "v_mul_f32_dpp v85, v11, v85 row_newbcast:3 row_mask:0xf bank_mask:0xf\n"
      "v_mfma_f32_16x16x32_bf16 v[52:55], v[244:247], v[24:27], v[52:55]\n"
      "s_waitcnt lgkmcnt(2)\n"
      "ds_read_b128 v[108:111], v7 offset:41984\n"
      "v_mfma_f32_16x16x32_bf16 v[48:51], v[248:251], v[28:31], v[48:51]\n"
      "v_mul_f32_dpp v82, v11, v82 row_newbcast:4 row_mask:0xf bank_mask:0xf\n"
      "v_mul_f32_dpp v86, v11, v86 row_newbcast:5 row_mask:0xf bank_mask:0xf\n"
      "v_mfma_f32_16x16x32_bf16 v[52:55], v[252:255], v[28:31], v[52:55]\n"
      "ds_read_b128 v[24:27], v6 offset:44032\n"
      "v_mul_f32_dpp v83, v11, v83 row_newbcast:6 row_mask:0xf bank_mask:0xf\n"
      "v_mfma_f32_16x16x32_bf16 v[56:59], v[112:115], v[32:35], v[56:59]\n"
      "v_mul_f32_dpp v87, v11, v87 row_newbcast:7 row_mask:0xf bank_mask:0xf\n"
      "v_cvt_pk_bf16_f32 v80, v80, v84\n"
      "v_mfma_f32_16x16x32_bf16 v[60:63], v[116:119], v[32:35], v[60:63]\n"
      "ds_read_b128 v[28:31], v7 offset:44032\n"
      "v_cvt_pk_bf16_f32 v81, v81, v85\n"
      "v_mfma_f32_16x16x32_bf16 v[56:59], v[120:123], v[36:39], v[56:59]\n"
      "v_cvt_pk_bf16_f32 v82, v82, v86\n"
      "v_cvt_pk_bf16_f32 v83, v83, v87\n"
      "v_mfma_f32_16x16x32_bf16 v[60:63], v[124:127], v[36:39], v[60:63]\n"
      "buffer_store_dwordx4 v[80:83], v3, s[16:19], s63 offen sc0 sc1 nt\n"
      "s_add_u32 s63, s63, 147456\n"
      "v_mfma_f32_16x16x32_bf16 v[56:59], v[128:131], v[40:43], v[56:59]\n"
      "ds_read_b128 v[32:35], v6 offset:46080\n"
      "v_fmac_f32_dpp v48, v12, v102 row_newbcast:0 row_mask:0xf bank_mask:0xf\n"
      "v_mfma_f32_16x16x32_bf16 v[60:63], v[132:135], v[40:43], v[60:63]\n"
      "s_waitcnt lgkmcnt(2)\n"
      "ds_read_b128 v[36:39], v7 offset:46080\n"
      "v_mfma_f32_16x16x32_bf16 v[56:59], v[136:139], v[44:47], v[56:59]\n"
      "v_fmac_f32_dpp v49, v12, v102 row_newbcast:2 row_mask:0xf bank_mask:0xf\n"
      "v_fmac_f32_dpp v50, v12, v102 row_newbcast:4 row_mask:0xf bank_mask:0xf\n"
      "v_mfma_f32_16x16x32_bf16 v[60:63], v[140:143], v[44:47], v[60:63]\n"
      "ds_read_b128 v[40:43], v6 offset:48128\n"
      "v_fmac_f32_dpp v51, v12, v102 row_newbcast:6 row_mask:0xf bank_mask:0xf\n"
      "v_mfma_f32_16x16x32_bf16 v[56:59], v[144:147], v[104:107], v[56:59]\n"
      "v_fmac_f32_dpp v52, v12, v102 row_newbcast:1 row_mask:0xf bank_mask:0xf\n"
      "v_fmac_f32_dpp v53, v12, v102 row_newbcast:3 row_mask:0xf bank_mask:0xf\n"
      "v_mfma_f32_16x16x32_bf16 v[60:63], v[148:151], v[104:107], v[60:63]\n"
      "ds_read_b128 v[44:47], v7 offset:48128\n"
      "v_fmac_f32_dpp v54, v12, v102 row_newbcast:5 row_mask:0xf bank_mask:0xf\n"
      "v_mfma_f32_16x16x32_bf16 v[56:59], v[152:155], v[108:111], v[56:59]\n"
      "v_fmac_f32_dpp v55, v12, v102 row_newbcast:7 row_mask:0xf bank_mask:0xf\n"
      "v_mul_f32_dpp v48, v11, v48 row_newbcast:0 row_mask:0xf bank_mask:0xf\n"
      "v_mfma_f32_16x16x32_bf16 v[60:63], v[156:159], v[108:111], v[60:63]\n"
      "ds_read_b128 v[104:107], v6 offset:50176\n"
      "v_mul_f32_dpp v52, v11, v52 row_newbcast:1 row_mask:0xf bank_mask:0xf\n"
      "v_mfma_f32_16x16x32_bf16 v[56:59], v[160:163], v[24:27], v[56:59]\n"
      "v_mul_f32_dpp v49, v11, v49 row_newbcast:2 row_mask:0xf bank_mask:0xf\n"
      "v_mul_f32_dpp v53, v11, v53 row_newbcast:3 row_mask:0xf bank_mask:0xf\n"
      "v_mfma_f32_16x16x32_bf16 v[60:63], v[164:167], v[24:27], v[60:63]\n"
      "s_waitcnt lgkmcnt(2)\n"
      "ds_read_b128 v[108:111], v7 offset:50176\n"
      "v_mfma_f32_16x16x32_bf16 v[56:59], v[168:171], v[28:31], v[56:59]\n"
      "v_mul_f32_dpp v50, v11, v50 row_newbcast:4 row_mask:0xf bank_mask:0xf\n"
      "v_mul_f32_dpp v54, v11, v54 row_newbcast:5 row_mask:0xf bank_mask:0xf\n"
      "v_mfma_f32_16x16x32_bf16 v[60:63], v[172:175], v[28:31], v[60:63]\n"
      "ds_read_b128 v[24:27], v6 offset:52224\n"
      "v_mul_f32_dpp v51, v11, v51 row_newbcast:6 row_mask:0xf bank_mask:0xf\n"
      "v_mfma_f32_16x16x32_bf16 v[56:59], v[176:179], v[32:35], v[56:59]\n"
      "v_mul_f32_dpp v55, v11, v55 row_newbcast:7 row_mask:0xf bank_mask:0xf\n"
      "v_cvt_pk_bf16_f32 v48, v48, v52\n"
      "v_mfma_f32_16x16x32_bf16 v[60:63], v[180:183], v[32:35], v[60:63]\n"
      "ds_read_b128 v[28:31], v7 offset:52224\n"
      "v_cvt_pk_bf16_f32 v49, v49, v53\n"
      "v_mfma_f32_16x16x32_bf16 v[56:59], v[184:187], v[36:39], v[56:59]\n"
      "v_cvt_pk_bf16_f32 v50, v50, v54\n"
      "v_cvt_pk_bf16_f32 v51, v51, v55\n"
      "v_mfma_f32_16x16x32_bf16 v[60:63], v[188:191], v[36:39], v[60:63]\n"
      "buffer_store_dwordx4 v[48:51], v3, s[16:19], s63 offen sc0 sc1 nt\n"
      "s_add_u32 s63, s63, 147456\n"
      "v_mfma_f32_16x16x32_bf16 v[56:59], v[192:195], v[40:43], v[56:59]\n"
      "ds_read_b128 v[32:35], v6 offset:54272\n"
      "ds_read_b128 v[36:39], v7 offset:54272\n"
      "v_mfma_f32_16x16x32_bf16 v[60:63], v[196:199], v[40:43], v[60:63]\n"
      "s_waitcnt lgkmcnt(3)\n"
      "v_mfma_f32_16x16x32_bf16 v[56:59], v[200:203], v[44:47], v[56:59]\n"
      "v_mfma_f32_16x16x32_bf16 v[60:63], v[204:207], v[44:47], v[60:63]\n"
      "v_mfma_f32_16x16x32_bf16 v[56:59], v[208:211], v[104:107], v[56:59]\n"
      "v_mfma_f32_16x16x32_bf16 v[60:63], v[212:215], v[104:107], v[60:63]\n"
      "v_mfma_f32_16x16x32_bf16 v[56:59], v[216:219], v[108:111], v[56:59]\n"
      "v_mfma_f32_16x16x32_bf16 v[60:63], v[220:223], v[108:111], v[60:63]\n"
      "v_mfma_f32_16x16x32_bf16 v[56:59], v[224:227], v[24:27], v[56:59]\n"
      "v_mfma_f32_16x16x32_bf16 v[60:63], v[228:231], v[24:27], v[60:63]\n"
      "s_waitcnt lgkmcnt(0)\n"
      "v_mfma_f32_16x16x32_bf16 v[56:59], v[232:235], v[28:31], v[56:59]\n"
      "v_mfma_f32_16x16x32_bf16 v[60:63], v[236:239], v[28:31], v[60:63]\n"
      "v_mfma_f32_16x16x32_bf16 v[56:59], v[240:243], v[32:35], v[56:59]\n"
      "v_mfma_f32_16x16x32_bf16 v[60:63], v[244:247], v[32:35], v[60:63]\n"
      "v_mfma_f32_16x16x32_bf16 v[56:59], v[248:251], v[36:39], v[56:59]\n"
      "v_mfma_f32_16x16x32_bf16 v[60:63], v[252:255], v[36:39], v[60:63]\n"
      "s_nop 6\n"
      "v_mul_f32_dpp v56, v11, v56 row_newbcast:0 row_mask:0xf bank_mask:0xf\n"
      "v_mul_f32_dpp v60, v11, v60 row_newbcast:1 row_mask:0xf bank_mask:0xf\n"
      "v_mul_f32_dpp v57, v11, v57 row_newbcast:2 row_mask:0xf bank_mask:0xf\n"
      "v_mul_f32_dpp v61, v11, v61 row_newbcast:3 row_mask:0xf bank_mask:0xf\n"
      "v_mul_f32_dpp v58, v11, v58 row_newbcast:4 row_mask:0xf bank_mask:0xf\n"
      "v_mul_f32_dpp v62, v11, v62 row_newbcast:5 row_mask:0xf bank_mask:0xf\n"
      "v_mul_f32_dpp v59, v11, v59 row_newbcast:6 row_mask:0xf bank_mask:0xf\n"
      "v_mul_f32_dpp v63, v11, v63 row_newbcast:7 row_mask:0xf bank_mask:0xf\n"
      "v_cvt_pk_bf16_f32 v56, v56, v60\n"
      "v_cvt_pk_bf16_f32 v57, v57, v61\n"
      "v_cvt_pk_bf16_f32 v58, v58, v62\n"
      "v_cvt_pk_bf16_f32 v59, v59, v63\n"
      "buffer_store_dwordx4 v[56:59], v3, s[16:19], s63 offen sc0 sc1 nt\n"
      ".Lfin%=:\n"
      "s_endpgm\n"
      ".Lend%=:\n"
      :
      : "s"(__builtin_amdgcn_kernarg_segment_ptr()), "s"(blockIdx.x), "v"(threadIdx.x), "s"(ldsbase)
      : "v1", "v2", "v3", "v4", "v5", "v6", "v7", "v8", "v9", "v10", "v11", "v12", "v13", "v14", "v15", "v16", "v17", "v18", "v19", "v20", "v21", "v22", "v23", "v24", "v25", "v26", "v27", "v28", "v29", "v30", "v31", "v32", "v33", "v34", "v35", "v36", "v37", "v38", "v39", "v40", "v41", "v42", "v43", "v44", "v45", "v46", "v47", "v48", "v49", "v50", "v51", "v52", "v53", "v54", "v55", "v56", "v57", "v58", "v59", "v60", "v61", "v62", "v63", "v64", "v65", "v66", "v67", "v68", "v69", "v70", "v71", "v72", "v73", "v74", "v75", "v76", "v77", "v78", "v79", "v80", "v81", "v82", "v83", "v84", "v85", "v86", "v87", "v88", "v89", "v90", "v91", "v92", "v93", "v94", "v95", "v96", "v97", "v98", "v99", "v100", "v101", "v102", "v103", "v104", "v105", "v106", "v107", "v108", "v109", "v110", "v111", "v112", "v113", "v114", "v115", "v116", "v117", "v118", "v119", "v120", "v121", "v122", "v123", "v124", "v125", "v126", "v127", "v128", "v129", "v130", "v131", "v132", "v133", "v134", "v135", "v136", "v137", "v138", "v139", "v140", "v141", "v142", "v143", "v144", "v145", "v146", "v147", "v148", "v149", "v150", "v151", "v152", "v153", "v154", "v155", "v156", "v157", "v158", "v159", "v160", "v161", "v162", "v163", "v164", "v165", "v166", "v167", "v168", "v169", "v170", "v171", "v172", "v173", "v174", "v175", "v176", "v177", "v178", "v179", "v180", "v181", "v182", "v183", "v184", "v185", "v186", "v187", "v188", "v189", "v190", "v191", "v192", "v193", "v194", "v195", "v196", "v197", "v198", "v199", "v200", "v201", "v202", "v203", "v204", "v205", "v206", "v207", "v208", "v209", "v210", "v211", "v212", "v213", "v214", "v215", "v216", "v217", "v218", "v219", "v220", "v221", "v222", "v223", "v224", "v225", "v226", "v227", "v228", "v229", "v230", "v231", "v232", "v233", "v234", "v235", "v236", "v237", "v238", "v239", "v240", "v241", "v242", "v243", "v244", "v245", "v246", "v247", "v248", "v249", "v250", "v251", "v252", "v253", "v254", "v255", "s8", "s9", "s10", "s11", "s12", "s13", "s14", "s15", "s16", "s17", "s18", "s19", "s20", "s21", "s22", "s23", "s24", "s25", "s26", "s27", "s28", "s29", "s30", "s31", "s32", "s33", "s34", "s35", "s36", "s37", "s38", "s39", "s40", "s41", "s42", "s43", "s44", "s45", "s46", "s47", "s48", "s49", "s50", "s51", "s52", "s53", "s54", "s55", "s56", "s57", "s58", "s59", "s60", "s61", "s62", "s63", "s64", "s65", "s66", "s67", "s68", "s69", "vcc", "scc", "m0", "memory");
}

extern "C" void kernel_launch(void* const* d_in, const int* in_sizes, int n_in, void* d_out, int out_size, void* d_ws,
                              size_t ws_size, hipStream_t stream) {
  gptq_gemm<<<256, 512, 0, stream>>>(d_in[0], 2363392u, 0x20000u, d_in[1], 1327104u, 0x20000u, d_out, 18874368u, 0x20000u, d_in[2], d_in[3]);
}
```
